```python
import torch

def _softmax(x, axis=-1):
    return torch.softmax(x, dim=axis)
def _gelu(x):
    return torch.nn.functional.gelu(x, approximate="tanh")
def _silu(x):
    return torch.nn.functional.silu(x)
def _sigmoid(x):
    return torch.sigmoid(x)
def _layernorm(x, axis=-1, eps=1e-5):
    m = x.mean(dim=axis, keepdim=True)
    v = x.var(dim=axis, keepdim=True, unbiased=False)
    return (x - m) / torch.sqrt(v + eps)
def _scatter_add(tgt, idx, src, axis=0):
    return tgt.clone().index_add_(axis, idx, src)
def _segment_sum(src, seg, n):
    out = torch.zeros((n,) + src.shape[1:], dtype=src.dtype)
    return out.index_add_(0, seg, src)
def _pad(x, pw):
    flat = tuple(v for pair in reversed(pw) for v in pair)
    return torch.nn.functional.pad(x, flat)

kernel_name = 'standalone__geglu_ffn__sampling'

def setup_inputs():

    _w = (lambda a: (lambda t: t.to(torch.bfloat16) if t.is_floating_point() else t)(torch.as_tensor(a)))
    B, S, D, F = 8, 8, 1024, 3584
    return {
        "x": _w((lambda *s: torch.randn(*s, dtype=torch.bfloat16) * 0.05)(B, S, D)),
        "w_gate": _w((lambda *s: torch.randn(*s, dtype=torch.bfloat16) * 0.05)(D, F)),
        "w_up": _w((lambda *s: torch.randn(*s, dtype=torch.bfloat16) * 0.05)(D, F)),
        "w_down": _w((lambda *s: torch.randn(*s, dtype=torch.bfloat16) * 0.05)(F, D)),
    }

def reference(x, w_gate, w_up, w_down):
    return (_gelu(x @ w_gate) * (x @ w_up)) @ w_down
```

```cpp
#include <hip/hip_runtime.h>
#include <hip/hip_bf16.h>

typedef short bf16x8 __attribute__((ext_vector_type(8)));
typedef float f32x4 __attribute__((ext_vector_type(4)));
typedef unsigned short u16;

constexpr int M = 64, D = 1024, F = 3584;

__device__ __forceinline__ u16 f2bf(float f) {
    __hip_bfloat16 b = __float2bfloat16(f);
    return __builtin_bit_cast(u16, b);
}
__device__ __forceinline__ float bfr(float f) {
    unsigned u = ((unsigned)f2bf(f)) << 16;
    return __builtin_bit_cast(float, u);
}
__device__ __forceinline__ float gelu_tanh(float x) {
    const float k0 = 0.7978845608028654f, k1 = 0.044715f;
    float inner = k0 * (x + k1 * x * x * x);
    return 0.5f * x * (1.0f + tanhf(inner));
}

__global__ __launch_bounds__(256) void k1(const u16* __restrict__ x, const u16* __restrict__ wg, const u16* __restrict__ wu, u16* __restrict__ h) {
    const int lane = threadIdx.x & 63, w = threadIdx.x >> 6;
    const int n0 = blockIdx.x * 16;
    f32x4 ag = {0, 0, 0, 0}, au = {0, 0, 0, 0};
    for (int k = 0; k < D; k += 32) {
        bf16x8 a = *(const bf16x8*)(x + (w * 16 + (lane & 15)) * D + k + 8 * (lane >> 4));
        bf16x8 bg, bu;
        for (int j = 0; j < 8; ++j) {
            bg[j] = wg[(size_t)(k + 8 * (lane >> 4) + j) * F + n0 + (lane & 15)];
            bu[j] = wu[(size_t)(k + 8 * (lane >> 4) + j) * F + n0 + (lane & 15)];
        }
        ag = __builtin_amdgcn_mfma_f32_16x16x32_bf16(a, bg, ag, 0, 0, 0);
        au = __builtin_amdgcn_mfma_f32_16x16x32_bf16(a, bu, au, 0, 0, 0);
    }
    for (int r = 0; r < 4; ++r) {
        int row = w * 16 + (lane >> 4) * 4 + r, col = n0 + (lane & 15);
        float g = bfr(gelu_tanh(bfr(ag[r])));
        h[row * F + col] = f2bf(g * bfr(au[r]));
    }
}

__global__ __launch_bounds__(256) void k2(const u16* __restrict__ h, const u16* __restrict__ wd, u16* __restrict__ out) {
    __shared__ float red[4][256];
    const int lane = threadIdx.x & 63, w = threadIdx.x >> 6;
    const int n0 = (blockIdx.x >> 2) * 16, m0 = (blockIdx.x & 3) * 16;
    f32x4 acc = {0, 0, 0, 0};
    for (int k = w * 896; k < (w + 1) * 896; k += 32) {
        bf16x8 a = *(const bf16x8*)(h + (m0 + (lane & 15)) * F + k + 8 * (lane >> 4));
        bf16x8 b;
        for (int j = 0; j < 8; ++j) b[j] = wd[(size_t)(k + 8 * (lane >> 4) + j) * D + n0 + (lane & 15)];
        acc = __builtin_amdgcn_mfma_f32_16x16x32_bf16(a, b, acc, 0, 0, 0);
    }
    for (int r = 0; r < 4; ++r) red[w][((lane >> 4) * 4 + r) * 16 + (lane & 15)] = acc[r];
    __syncthreads();
    int t = threadIdx.x;
    float s = red[0][t] + red[1][t] + red[2][t] + red[3][t];
    out[(m0 + (t >> 4)) * D + n0 + (t & 15)] = f2bf(s);
}

extern "C" void kernel_launch(void* const* d_in, const int* in_sizes, int n_in,
                              void* d_out, int out_size, void* d_ws, size_t ws_size,
                              hipStream_t stream) {
    const u16* x = (const u16*)d_in[0];
    const u16* wg = (const u16*)d_in[1];
    const u16* wu = (const u16*)d_in[2];
    const u16* wd = (const u16*)d_in[3];
    u16* h = (u16*)d_ws;
    k1<<<F / 16, 256, 0, stream>>>(x, wg, wu, h);
    k2<<<256, 256, 0, stream>>>(h, wd, (u16*)d_out);
}
```

```cpp
#include <hip/hip_runtime.h>
#include <hip/hip_bf16.h>

typedef short bf16x8 __attribute__((ext_vector_type(8)));
typedef float f32x4 __attribute__((ext_vector_type(4)));
typedef unsigned short u16;

constexpr int M = 64, D = 1024, F = 3584;

__device__ __forceinline__ u16 f2bf(float f) {
    __hip_bfloat16 b = __float2bfloat16(f);
    return __builtin_bit_cast(u16, b);
}
__device__ __forceinline__ float bfr(float f) {
    unsigned u = ((unsigned)f2bf(f)) << 16;
    return __builtin_bit_cast(float, u);
}
__device__ __forceinline__ float gelu_tanh(float x) {
    const float k0 = 0.7978845608028654f, k1 = 0.044715f;
    float inner = k0 * (x + k1 * x * x * x);
    return 0.5f * x * (1.0f + tanhf(inner));
}

__global__ __launch_bounds__(256) void k1(const u16* __restrict__ x, const u16* __restrict__ wg, const u16* __restrict__ wu, u16* __restrict__ h) {
    const int lane = threadIdx.x & 63, w = threadIdx.x >> 6;
    const int n0 = blockIdx.x * 16;
    f32x4 ag = {0, 0, 0, 0}, au = {0, 0, 0, 0};
    for (int k = 0; k < D; k += 32) {
        bf16x8 a = *(const bf16x8*)(x + (w * 16 + (lane & 15)) * D + k + 8 * (lane >> 4));
        bf16x8 bg, bu;
        for (int j = 0; j < 8; ++j) {
            bg[j] = wg[(size_t)(k + 8 * (lane >> 4) + j) * F + n0 + (lane & 15)];
            bu[j] = wu[(size_t)(k + 8 * (lane >> 4) + j) * F + n0 + (lane & 15)];
        }
        ag = __builtin_amdgcn_mfma_f32_16x16x32_bf16(a, bg, ag, 0, 0, 0);
        au = __builtin_amdgcn_mfma_f32_16x16x32_bf16(a, bu, au, 0, 0, 0);
    }
    for (int r = 0; r < 4; ++r) {
        int row = w * 16 + (lane >> 4) * 4 + r, col = n0 + (lane & 15);
        float g = bfr(gelu_tanh(bfr(ag[r])));
        h[row * F + col] = f2bf(g * bfr(au[r]));
    }
}

__global__ __launch_bounds__(256) void k2(const u16* __restrict__ h, const u16* __restrict__ wd, u16* __restrict__ out) {
    __shared__ float red[4][256];
    const int lane = threadIdx.x & 63, w = threadIdx.x >> 6;
    const int n0 = (blockIdx.x >> 2) * 16, m0 = (blockIdx.x & 3) * 16;
    f32x4 acc = {0, 0, 0, 0};
    for (int k = w * 896; k < (w + 1) * 896; k += 32) {
        bf16x8 a = *(const bf16x8*)(h + (m0 + (lane & 15)) * F + k + 8 * (lane >> 4));
        bf16x8 b;
        for (int j = 0; j < 8; ++j) b[j] = wd[(size_t)(k + 8 * (lane >> 4) + j) * D + n0 + (lane & 15)];
        acc = __builtin_amdgcn_mfma_f32_16x16x32_bf16(a, b, acc, 0, 0, 0);
    }
    for (int r = 0; r < 4; ++r) red[w][((lane >> 4) * 4 + r) * 16 + (lane & 15)] = acc[r];
    __syncthreads();
    int t = threadIdx.x;
    float s = red[0][t] + red[1][t] + red[2][t] + red[3][t];
    out[(m0 + (t >> 4)) * D + n0 + (t & 15)] = f2bf(s);
}


typedef unsigned v4u __attribute__((ext_vector_type(4)));
typedef unsigned long long u64;
constexpr int WB = D * F * 2;
__device__ __forceinline__ u64 rt() { return __builtin_amdgcn_s_memrealtime(); }
template <int AUX> __device__ __forceinline__ v4u ld(__amdgpu_buffer_rsrc_t rs, int off) { return __builtin_amdgcn_raw_buffer_load_b128(rs, off, 0, AUX); }
constexpr int AMP = 100;
__device__ __forceinline__ void amp(u64 d) { if (d > 2000) d = 2000; u64 e = rt() + d * AMP; while (rt() < e) __builtin_amdgcn_s_sleep(2); }

template <int AUX>
__global__ __launch_bounds__(512) void probe_contig(const char* wg, const char* wu, const char* wd, unsigned* sink) {
    extern __shared__ char smem[];
    const int tid = threadIdx.x, b = blockIdx.x;
    u64 t0 = rt();
    auto rg = __builtin_amdgcn_make_buffer_rsrc((void*)wg, 0, WB, 0x00020000);
    auto ru = __builtin_amdgcn_make_buffer_rsrc((void*)wu, 0, WB, 0x00020000);
    auto rd = __builtin_amdgcn_make_buffer_rsrc((void*)wd, 0, WB, 0x00020000);
    v4u v[12];
#pragma unroll
    for (int i = 0; i < 4; ++i) { int c = i * 512 + tid; int off = c < 1792 ? b * 28672 + c * 16 : 0x7ffffff0; v[i] = ld<AUX>(rg, off); v[4 + i] = ld<AUX>(ru, off); v[8 + i] = ld<AUX>(rd, off); }
    v4u acc = {0, 0, 0, 0};
#pragma unroll
    for (int i = 0; i < 12; ++i) acc ^= v[i];
    asm volatile("" :: "v"(acc));
    u64 t1 = rt();
    amp(t1 - t0);
}
template <int AUX0, int AUX1>
__global__ __launch_bounds__(512) void probe_rot(const char* wg, const char* wu, unsigned* sink) {
    extern __shared__ char smem[];
    const int tid = threadIdx.x, lane = tid & 63, w = tid >> 6, b = blockIdx.x;
    u64 t0 = rt();
    auto rg = __builtin_amdgcn_make_buffer_rsrc((void*)wg, 0, WB, 0x00020000);
    auto ru = __builtin_amdgcn_make_buffer_rsrc((void*)wu, 0, WB, 0x00020000);
    v4u acc = {0, 0, 0, 0};
    const int xcd = b & 7, r = b >> 3;
    v4u vw[32];
    if (r < 28) {
        const int lid = xcd * 28 + r, ms = lid & 3, cg = lid >> 2;
#pragma unroll
        for (int i = 0; i < 8; ++i) { int row = ms * 256 + ((i >> 1) * 8 + w) * 8 + (lane >> 3); vw[i] = ld<AUX0>((i & 1) ? ru : rg, row * (F * 2) + cg * 128 + (lane & 7) * 16); }
        __builtin_amdgcn_sched_barrier(0);
        asm volatile("s_waitcnt vmcnt(0)" ::: "memory");
        while (rt() < t0 + 1200) __builtin_amdgcn_s_sleep(1);
        u64 tm = rt();
        __builtin_amdgcn_sched_barrier(0);
#pragma unroll
        for (int i = 8; i < 32; ++i) { int q = (ms + (i >> 3)) & 3; int row = q * 256 + (((i & 7) >> 1) * 8 + w) * 8 + (lane >> 3); vw[i] = ld<AUX1>((i & 1) ? ru : rg, row * (F * 2) + cg * 128 + (lane & 7) * 16); }
        __builtin_amdgcn_sched_barrier(0);
#pragma unroll
        for (int i = 0; i < 32; ++i) acc ^= vw[i];
        asm volatile("" :: "v"(acc));
        u64 t1 = rt();
        amp(t1 - tm);
    }
}
__global__ __launch_bounds__(512) void probe_stampA(u64* st) { extern __shared__ char smem[]; u64 t0 = rt(); if (threadIdx.x == 0) st[blockIdx.x] = t0; }
__global__ __launch_bounds__(64) void probe_stampB(const u64* st) { u64 mn = ~0ull, mx = 0; for (int i = 0; i < 256; ++i) { u64 v = st[i]; mn = v < mn ? v : mn; mx = v > mx ? v : mx; } amp(mx - mn); }
__global__ __launch_bounds__(64) void probe_lat(const char* p) {
    auto rs = __builtin_amdgcn_make_buffer_rsrc((void*)p, 0, 131072, 0x00020000);
    v4u a = ld<0>(rs, 65536 + blockIdx.x * 128);
    asm volatile("s_waitcnt vmcnt(0)" ::: "memory");
    asm volatile("" :: "v"(a));
    u64 t0 = rt();
    v4u v = ld<0>(rs, blockIdx.x * 1024 + threadIdx.x * 16);
    asm volatile("" :: "v"(v));
    u64 t1 = rt();
    amp(t1 - t0);
}
extern "C" void kernel_launch(void* const* d_in, const int* in_sizes, int n_in,
                              void* d_out, int out_size, void* d_ws, size_t ws_size,
                              hipStream_t stream) {
    const u16* x = (const u16*)d_in[0];
    const u16* wg = (const u16*)d_in[1];
    const u16* wu = (const u16*)d_in[2];
    const u16* wd = (const u16*)d_in[3];
    u16* h = (u16*)d_ws;
    hipFuncSetAttribute((const void*)probe_stampA, hipFuncAttributeMaxDynamicSharedMemorySize, 98304); probe_stampA<<<256, 512, 98304, stream>>>((u64*)d_ws + (1<<20)); probe_stampB<<<1, 64, 0, stream>>>((const u64*)d_ws + (1<<20));
    k1<<<F / 16, 256, 0, stream>>>(x, wg, wu, h);
    k2<<<256, 256, 0, stream>>>(h, wd, (u16*)d_out);
}
```

```cpp
#include <hip/hip_runtime.h>
#include <hip/hip_bf16.h>

typedef short bf16x8 __attribute__((ext_vector_type(8)));
typedef float f32x4 __attribute__((ext_vector_type(4)));
typedef unsigned short u16;

constexpr int M = 64, D = 1024, F = 3584;

__device__ __forceinline__ u16 f2bf(float f) {
    __hip_bfloat16 b = __float2bfloat16(f);
    return __builtin_bit_cast(u16, b);
}
__device__ __forceinline__ float bfr(float f) {
    unsigned u = ((unsigned)f2bf(f)) << 16;
    return __builtin_bit_cast(float, u);
}
__device__ __forceinline__ float gelu_tanh(float x) {
    const float k0 = 0.7978845608028654f, k1 = 0.044715f;
    float inner = k0 * (x + k1 * x * x * x);
    return 0.5f * x * (1.0f + tanhf(inner));
}

__global__ __launch_bounds__(256) void k1(const u16* __restrict__ x, const u16* __restrict__ wg, const u16* __restrict__ wu, u16* __restrict__ h) {
    const int lane = threadIdx.x & 63, w = threadIdx.x >> 6;
    const int n0 = blockIdx.x * 16;
    f32x4 ag = {0, 0, 0, 0}, au = {0, 0, 0, 0};
    for (int k = 0; k < D; k += 32) {
        bf16x8 a = *(const bf16x8*)(x + (w * 16 + (lane & 15)) * D + k + 8 * (lane >> 4));
        bf16x8 bg, bu;
        for (int j = 0; j < 8; ++j) {
            bg[j] = wg[(size_t)(k + 8 * (lane >> 4) + j) * F + n0 + (lane & 15)];
            bu[j] = wu[(size_t)(k + 8 * (lane >> 4) + j) * F + n0 + (lane & 15)];
        }
        ag = __builtin_amdgcn_mfma_f32_16x16x32_bf16(a, bg, ag, 0, 0, 0);
        au = __builtin_amdgcn_mfma_f32_16x16x32_bf16(a, bu, au, 0, 0, 0);
    }
    for (int r = 0; r < 4; ++r) {
        int row = w * 16 + (lane >> 4) * 4 + r, col = n0 + (lane & 15);
        float g = bfr(gelu_tanh(bfr(ag[r])));
        h[row * F + col] = f2bf(g * bfr(au[r]));
    }
}

__global__ __launch_bounds__(256) void k2(const u16* __restrict__ h, const u16* __restrict__ wd, u16* __restrict__ out) {
    __shared__ float red[4][256];
    const int lane = threadIdx.x & 63, w = threadIdx.x >> 6;
    const int n0 = (blockIdx.x >> 2) * 16, m0 = (blockIdx.x & 3) * 16;
    f32x4 acc = {0, 0, 0, 0};
    for (int k = w * 896; k < (w + 1) * 896; k += 32) {
        bf16x8 a = *(const bf16x8*)(h + (m0 + (lane & 15)) * F + k + 8 * (lane >> 4));
        bf16x8 b;
        for (int j = 0; j < 8; ++j) b[j] = wd[(size_t)(k + 8 * (lane >> 4) + j) * D + n0 + (lane & 15)];
        acc = __builtin_amdgcn_mfma_f32_16x16x32_bf16(a, b, acc, 0, 0, 0);
    }
    for (int r = 0; r < 4; ++r) red[w][((lane >> 4) * 4 + r) * 16 + (lane & 15)] = acc[r];
    __syncthreads();
    int t = threadIdx.x;
    float s = red[0][t] + red[1][t] + red[2][t] + red[3][t];
    out[(m0 + (t >> 4)) * D + n0 + (t & 15)] = f2bf(s);
}


typedef unsigned v4u __attribute__((ext_vector_type(4)));
typedef unsigned long long u64;
constexpr int WB = D * F * 2;
__device__ __forceinline__ u64 rt() { return __builtin_amdgcn_s_memrealtime(); }
template <int AUX> __device__ __forceinline__ v4u ld(__amdgpu_buffer_rsrc_t rs, int off) { return __builtin_amdgcn_raw_buffer_load_b128(rs, off, 0, AUX); }
constexpr int AMP = 100;
__device__ __forceinline__ void amp(u64 d) { if (d > 2000) d = 2000; u64 e = rt() + d * AMP; while (rt() < e) __builtin_amdgcn_s_sleep(2); }

__global__ __launch_bounds__(64) void probe_karg(const char* p) {
    u64 t0, t1;
    asm volatile("s_memrealtime %0\n\ts_waitcnt lgkmcnt(0)" : "=s"(t0) :: "memory");
    __builtin_amdgcn_sched_barrier(0);
    asm volatile("s_load_dwordx2 s[40:41], s[0:1], 0x0\n\ts_waitcnt lgkmcnt(0)\n\ts_memrealtime %0\n\ts_waitcnt lgkmcnt(0)" : "=s"(t1) :: "memory", "s40", "s41");
    amp(t1 - t0);
    if (t1 == 12345) *(char*)p = 0;
}
template <int N>
__global__ __launch_bounds__(64) void probe_code() {
    u64 t0, t1;
    asm volatile("s_memrealtime %0\n\ts_waitcnt lgkmcnt(0)" : "=s"(t0) :: "memory");
    asm volatile(".rept %c0\n\tv_mov_b32 v1, 0x12345678\n\t.endr" :: "i"(N) : "v1");
    asm volatile("s_memrealtime %0\n\ts_waitcnt lgkmcnt(0)" : "=s"(t1) :: "memory");
    amp(t1 - t0);
}
__global__ __launch_bounds__(64) void probe_lat(const char* p) {
    auto rs = __builtin_amdgcn_make_buffer_rsrc((void*)p, 0, WB, 0x00020000);
    asm volatile("" :: "s"(rs));
    u64 t0;
    asm volatile("s_waitcnt lgkmcnt(0)\n\ts_memrealtime %0\n\ts_waitcnt lgkmcnt(0)" : "=s"(t0) :: "memory");
    v4u v = ld<0>(rs, blockIdx.x * 28672 + threadIdx.x * 16);
    asm volatile("" :: "v"(v));
    u64 t1 = rt();
    amp(t1 - t0);
}
template <int SEG, int AUX>
__global__ __launch_bounds__(512) void probe_seg(const char* wg, const char* wu) {
    extern __shared__ char smem[];
    const int tid = threadIdx.x, lane = tid & 63, w = tid >> 6, b = blockIdx.x;
    u64 t0 = rt();
    auto rg = __builtin_amdgcn_make_buffer_rsrc((void*)wg, 0, WB, 0x00020000);
    auto ru = __builtin_amdgcn_make_buffer_rsrc((void*)wu, 0, WB, 0x00020000);
    const int xcd = b & 7, r = b >> 3, lid = xcd * 28 + r;
    constexpr int KR = 32768 / SEG, NK = 1024 / KR, LPR = SEG / 16;
    v4u acc = {0, 0, 0, 0};
    if (r < 28) {
        const int kg = lid % NK, cg = lid / NK;
        v4u vw[8];
#pragma unroll
        for (int i = 0; i < 8; ++i) { int c = ((i >> 1) * 8 + w) * 64 + lane; int row = kg * KR + c / LPR; vw[i] = ld<AUX>((i & 1) ? ru : rg, row * (F * 2) + cg * SEG + (c % LPR) * 16); }
#pragma unroll
        for (int i = 0; i < 8; ++i) acc ^= vw[i];
    }
    asm volatile("" :: "v"(acc));
    u64 t1 = rt();
    amp(t1 - t0);
}
extern "C" void kernel_launch(void* const* d_in, const int* in_sizes, int n_in,
                              void* d_out, int out_size, void* d_ws, size_t ws_size,
                              hipStream_t stream) {
    const u16* x = (const u16*)d_in[0];
    const u16* wg = (const u16*)d_in[1];
    const u16* wu = (const u16*)d_in[2];
    const u16* wd = (const u16*)d_in[3];
    u16* h = (u16*)d_ws;
    probe_karg<<<256, 64, 0, stream>>>((const char*)d_ws);
    k1<<<F / 16, 256, 0, stream>>>(x, wg, wu, h);
    k2<<<256, 256, 0, stream>>>(h, wd, (u16*)d_out);
}
```

```cpp
#define PADT 160000
#include <hip/hip_runtime.h>
#include <hip/hip_bf16.h>
typedef short bf16x8 __attribute__((ext_vector_type(8)));
typedef float f32x4 __attribute__((ext_vector_type(4)));
typedef unsigned short u16;
typedef unsigned long long u64;
constexpr int M = 64, D = 1024, F = 3584;
__device__ __forceinline__ u16 f2bf(float f) { __hip_bfloat16 b = __float2bfloat16(f); return __builtin_bit_cast(u16, b); }
__device__ __forceinline__ float bfr(float f) { unsigned u = ((unsigned)f2bf(f)) << 16; return __builtin_bit_cast(float, u); }
__device__ __forceinline__ float gelu_tanh(float x) { const float k0 = 0.7978845608028654f, k1 = 0.044715f; return 0.5f * x * (1.0f + tanhf(k0 * (x + k1 * x * x * x))); }
__device__ __forceinline__ u64 rt() { return __builtin_amdgcn_s_memrealtime(); }
__global__ __launch_bounds__(512) void kall(const u16* __restrict__ x, const u16* __restrict__ wg, const u16* __restrict__ wu, const u16* __restrict__ wd, u16* __restrict__ out) {
    extern __shared__ __attribute__((aligned(16))) u16 hs[];
    u64 t0 = rt();
    const int lane = threadIdx.x & 63, w = threadIdx.x >> 6;
    if (blockIdx.x < 4) {
        const int m0 = blockIdx.x * 16;
        for (int n0 = w * 16; n0 < F; n0 += 128) {
            f32x4 ag = {0, 0, 0, 0}, au = {0, 0, 0, 0};
            for (int k = 0; k < D; k += 32) {
                bf16x8 a = *(const bf16x8*)(x + (m0 + (lane & 15)) * D + k + 8 * (lane >> 4));
                bf16x8 bg, bu;
                for (int j = 0; j < 8; ++j) { bg[j] = wg[(size_t)(k + 8 * (lane >> 4) + j) * F + n0 + (lane & 15)]; bu[j] = wu[(size_t)(k + 8 * (lane >> 4) + j) * F + n0 + (lane & 15)]; }
                ag = __builtin_amdgcn_mfma_f32_16x16x32_bf16(a, bg, ag, 0, 0, 0);
                au = __builtin_amdgcn_mfma_f32_16x16x32_bf16(a, bu, au, 0, 0, 0);
            }
            for (int r = 0; r < 4; ++r) { int row = (lane >> 4) * 4 + r, col = n0 + (lane & 15); hs[row * F + col] = f2bf(bfr(gelu_tanh(bfr(ag[r]))) * bfr(au[r])); }
        }
        __syncthreads();
        for (int n0 = w * 16; n0 < D; n0 += 128) {
            f32x4 acc = {0, 0, 0, 0};
            for (int k = 0; k < F; k += 32) {
                bf16x8 a = *(const bf16x8*)(hs + (lane & 15) * F + k + 8 * (lane >> 4));
                bf16x8 b;
                for (int j = 0; j < 8; ++j) b[j] = wd[(size_t)(k + 8 * (lane >> 4) + j) * D + n0 + (lane & 15)];
                acc = __builtin_amdgcn_mfma_f32_16x16x32_bf16(a, b, acc, 0, 0, 0);
            }
            for (int r = 0; r < 4; ++r) out[(m0 + (lane >> 4) * 4 + r) * D + n0 + (lane & 15)] = f2bf(acc[r]);
        }
    }
    while (rt() < t0 + PADT) __builtin_amdgcn_s_sleep(1);
}
__global__ void knop() {}
extern "C" void kernel_launch(void* const* d_in, const int* in_sizes, int n_in, void* d_out, int out_size, void* d_ws, size_t ws_size, hipStream_t stream) {
    hipFuncSetAttribute((const void*)kall, hipFuncAttributeMaxDynamicSharedMemorySize, 16 * F * 2);
    kall<<<256, 512, 16 * F * 2, stream>>>((const u16*)d_in[0], (const u16*)d_in[1], (const u16*)d_in[2], (const u16*)d_in[3], (u16*)d_out);
}
```

```cpp
#include <hip/hip_runtime.h>
#include <hip/hip_bf16.h>
typedef short bf16x8 __attribute__((ext_vector_type(8)));
typedef float f32x4 __attribute__((ext_vector_type(4)));
typedef unsigned short u16;
typedef unsigned long long u64;
constexpr int M = 64, D = 1024, F = 3584;
__device__ __forceinline__ u16 f2bf(float f) { __hip_bfloat16 b = __float2bfloat16(f); return __builtin_bit_cast(u16, b); }
__device__ __forceinline__ float bfr(float f) { unsigned u = ((unsigned)f2bf(f)) << 16; return __builtin_bit_cast(float, u); }
__device__ __forceinline__ float gelu_tanh(float x) { const float k0 = 0.7978845608028654f, k1 = 0.044715f; return 0.5f * x * (1.0f + tanhf(k0 * (x + k1 * x * x * x))); }
__device__ __forceinline__ u64 rt() { return __builtin_amdgcn_s_memrealtime(); }
#define PADT 200000
extern "C" __global__ __launch_bounds__(512) void kall(const u16* __restrict__ x, const u16* __restrict__ wg, const u16* __restrict__ wu, const u16* __restrict__ wd, u16* __restrict__ out) {
    extern __shared__ __attribute__((aligned(16))) u16 hs[];
    asm volatile("" :: "s"(x), "s"(wg), "s"(wu), "s"(wd), "s"(out));
    u64 t0; asm volatile("s_waitcnt lgkmcnt(0)\n\ts_memrealtime %0\n\ts_waitcnt lgkmcnt(0)" : "=s"(t0) :: "memory");
    const int lane = threadIdx.x & 63, w = threadIdx.x >> 6;
    if (blockIdx.x < 4) {
        const int m0 = blockIdx.x * 16;
        for (int n0 = w * 16; n0 < F; n0 += 128) {
            f32x4 ag = {0, 0, 0, 0}, au = {0, 0, 0, 0};
            for (int k = 0; k < D; k += 32) {
                bf16x8 a = *(const bf16x8*)(x + (m0 + (lane & 15)) * D + k + 8 * (lane >> 4));
                bf16x8 bg, bu;
                for (int j = 0; j < 8; ++j) { bg[j] = wg[(size_t)(k + 8 * (lane >> 4) + j) * F + n0 + (lane & 15)]; bu[j] = wu[(size_t)(k + 8 * (lane >> 4) + j) * F + n0 + (lane & 15)]; }
                ag = __builtin_amdgcn_mfma_f32_16x16x32_bf16(a, bg, ag, 0, 0, 0);
                au = __builtin_amdgcn_mfma_f32_16x16x32_bf16(a, bu, au, 0, 0, 0);
            }
            for (int r = 0; r < 4; ++r) { int row = (lane >> 4) * 4 + r, col = n0 + (lane & 15); hs[row * F + col] = f2bf(bfr(gelu_tanh(bfr(ag[r]))) * bfr(au[r])); }
        }
        __syncthreads();
        for (int n0 = w * 16; n0 < D; n0 += 128) {
            f32x4 acc = {0, 0, 0, 0};
            for (int k = 0; k < F; k += 32) {
                bf16x8 a = *(const bf16x8*)(hs + (lane & 15) * F + k + 8 * (lane >> 4));
                bf16x8 b;
                for (int j = 0; j < 8; ++j) b[j] = wd[(size_t)(k + 8 * (lane >> 4) + j) * D + n0 + (lane & 15)];
                acc = __builtin_amdgcn_mfma_f32_16x16x32_bf16(a, b, acc, 0, 0, 0);
            }
            for (int r = 0; r < 4; ++r) out[(m0 + (lane >> 4) * 4 + r) * D + n0 + (lane & 15)] = f2bf(acc[r]);
        }
    }
    while (rt() < t0 + PADT) __builtin_amdgcn_s_sleep(1);
}
extern "C" void kernel_launch(void* const* d_in, const int* in_sizes, int n_in, void* d_out, int out_size, void* d_ws, size_t ws_size, hipStream_t stream) {
    hipFuncSetAttribute((const void*)kall, hipFuncAttributeMaxDynamicSharedMemorySize, 16 * F * 2);
    kall<<<256, 512, 16 * F * 2, stream>>>((const u16*)d_in[0], (const u16*)d_in[1], (const u16*)d_in[2], (const u16*)d_in[3], (u16*)d_out);
}
```

```cpp
#include <hip/hip_runtime.h>

typedef unsigned short u16;
typedef unsigned long long u64;
typedef unsigned v4u __attribute__((ext_vector_type(4)));
constexpr int D = 1024, F = 3584;
constexpr int WBYTES = D * F * 2;
constexpr int LDS_BYTES = 147456;

__global__ __launch_bounds__(512, 2) void geglu_ffn(const u16* x, const u16* wg, const u16* wu, const u16* wd, u16* out, char* ws) {
    asm volatile(
        "s_memrealtime s[68:69]\n"
        "s_load_dwordx2 s[8:9], %0, 0x8\n"
        "s_load_dwordx2 s[12:13], %0, 0x10\n"
        "s_load_dwordx2 s[4:5], %0, 0x0\n"
        "s_load_dwordx2 s[16:17], %0, 0x18\n"
        "s_load_dwordx2 s[20:21], %0, 0x20\n"
        "s_load_dwordx2 s[24:25], %0, 0x28\n"
        "v_readfirstlane_b32 s30, %2\n"
        "s_and_b32 s31, %1, 7\n"
        "s_lshr_b32 s3, %1, 3\n"
        "v_and_b32 v1, 63, %2\n"
        "s_lshr_b32 s30, s30, 6\n"
        "s_mov_b32 s10, 7340032\n"
        "s_mov_b32 s11, 0x20000\n"
        "s_mov_b32 s6, s11\n"
        "s_mov_b32 s7, s11\n"
        "s_cmp_lt_u32 s3, 14\n"
        "s_cbranch_scc1 P1_%=\n"
        "s_cmp_lg_u32 s30, 0\n"
        "s_cbranch_scc1 HELP_OTHER_%=\n"
        "s_sub_u32 s56, s3, 14\n"
        "v_mul_u32_u24 v2, 37, v1\n"
        "s_lshr_b32 s57, s56, 1\n"
        "v_lshrrev_b32 v2, 8, v2\n"
        "s_mul_i32 s57, s57, 64512\n"
        "v_mul_u32_u24 v3, 6272, v2\n"
        "s_mul_i32 s58, s31, 896\n"
        "v_lshl_add_u32 v3, v1, 7, v3\n"
        "s_add_u32 s57, s57, s58\n"
        "v_bfrev_b32 v4, 14\n"
        "v_add_u32 v3, s57, v3\n"
        "v_cmp_eq_u32 vcc, 63, v1\n"
        "s_mov_b32 s59, 580608\n"
        "s_lshl_b32 s58, s56, 13\n"
        "v_cndmask_b32 v3, v3, v4, vcc\n"
        "v_lshl_add_u32 v4, v1, 7, s58\n"
        "s_bitcmp1_b32 s56, 0\n"
        "s_waitcnt lgkmcnt(0)\n"
        "buffer_load_dword v151, v4, s[4:7], 0 offen\n"
        "s_cselect_b64 s[8:9], s[12:13], s[8:9]\n"
        "buffer_load_dword v151, v3, s[8:11], 0 offen\n"
        "v_add_u32 v3, s59, v3\n"
        "buffer_load_dword v151, v3, s[8:11], 0 offen\n"
        "v_add_u32 v3, s59, v3\n"
        "buffer_load_dword v151, v3, s[8:11], 0 offen\n"
        "v_add_u32 v3, s59, v3\n"
        "buffer_load_dword v151, v3, s[8:11], 0 offen\n"
        "v_add_u32 v3, s59, v3\n"
        "buffer_load_dword v151, v3, s[8:11], 0 offen\n"
        "v_add_u32 v3, s59, v3\n"
        "buffer_load_dword v151, v3, s[8:11], 0 offen\n"
        "v_add_u32 v3, s59, v3\n"
        "buffer_load_dword v151, v3, s[8:11], 0 offen\n"
        "v_add_u32 v3, s59, v3\n"
        "buffer_load_dword v151, v3, s[8:11], 0 offen\n"
        "v_add_u32 v3, s59, v3\n"
        "buffer_load_dword v151, v3, s[8:11], 0 offen\n"
        "v_add_u32 v3, s59, v3\n"
        "buffer_load_dword v151, v3, s[8:11], 0 offen\n"
        "v_add_u32 v3, s59, v3\n"
        "buffer_load_dword v151, v3, s[8:11], 0 offen\n"
        "v_add_u32 v3, s59, v3\n"
        "buffer_load_dword v151, v3, s[8:11], 0 offen\n"
        "v_add_u32 v3, s59, v3\n"
        "buffer_load_dword v151, v3, s[8:11], 0 offen\n"
        "s_branch SETUP_%=\n"
        "HELP_OTHER_%=:\n"
        "s_cmp_lg_u32 s30, 1\n"
        "s_cbranch_scc1 SETUP_%=\n"
        "s_cmp_lt_u32 s3, 30\n"
        "s_cbranch_scc1 SETUP_%=\n"
        "s_getpc_b64 s[56:57]\n"
        "CW_PC_%=:\n"
        "v_lshlrev_b32 v2, 7, v1\n"
        "s_sub_u32 s58, GEGLU_END_%=-CW_PC_%=, 4\n"
        "v_min_u32 v2, s58, v2\n"
        "global_load_dword v151, v2, s[56:57]\n"
        "s_branch SETUP_%=\n"
        "P1_%=:\n"
        "s_cmp_ge_u32 s30, 4\n"
        "s_cbranch_scc1 SETUP_%=\n"
        "v_lshrrev_b32 v2, 3, v1\n"
        "v_and_b32 v3, 7, v1\n"
        "v_lshrrev_b32 v4, 4, v1\n"
        "s_lshl_b32 s60, s30, 2\n"
        "s_and_b32 s60, s60, 4\n"
        "v_or_b32 v4, s60, v4\n"
        "v_xor_b32 v4, v4, v3\n"
        "v_lshlrev_b32 v4, 4, v4\n"
        "v_lshl_or_b32 v4, v2, 11, v4\n"
        "s_and_b32 s61, s3, 1\n"
        "s_lshl_b32 s61, s61, 16\n"
        "s_lshl_b32 s62, s30, 14\n"
        "s_add_u32 s61, s61, s62\n"
        "s_lshl_b32 m0, s30, 10\n"
        "s_movk_i32 s62, 0x1000\n"
        "s_waitcnt lgkmcnt(0)\n"
        "buffer_load_dwordx4 v4, s[4:7], s61 offen lds\n"
        "s_add_u32 m0, m0, s62\n"
        "s_addk_i32 s61, 0x80\n"
        "buffer_load_dwordx4 v4, s[4:7], s61 offen lds\n"
        "s_add_u32 m0, m0, s62\n"
        "s_addk_i32 s61, 0x80\n"
        "buffer_load_dwordx4 v4, s[4:7], s61 offen lds\n"
        "s_add_u32 m0, m0, s62\n"
        "s_addk_i32 s61, 0x80\n"
        "buffer_load_dwordx4 v4, s[4:7], s61 offen lds\n"
        "s_add_u32 m0, m0, s62\n"
        "s_addk_i32 s61, 0x80\n"
        "buffer_load_dwordx4 v4, s[4:7], s61 offen lds\n"
        "s_add_u32 m0, m0, s62\n"
        "s_addk_i32 s61, 0x80\n"
        "buffer_load_dwordx4 v4, s[4:7], s61 offen lds\n"
        "s_add_u32 m0, m0, s62\n"
        "s_addk_i32 s61, 0x80\n"
        "buffer_load_dwordx4 v4, s[4:7], s61 offen lds\n"
        "s_add_u32 m0, m0, s62\n"
        "s_addk_i32 s61, 0x80\n"
        "buffer_load_dwordx4 v4, s[4:7], s61 offen lds\n"
        "s_add_u32 m0, m0, s62\n"
        "s_addk_i32 s61, 0x80\n"
        "SETUP_%=:\n"
        "s_cmp_ge_u32 s3, 3\n"
        "s_addc_u32 s33, s3, 0\n"
        "s_cmp_ge_u32 s3, 10\n"
        "s_addc_u32 s33, s33, 0\n"
        "s_sub_u32 s56, s3, 14\n"
        "s_cmp_lt_u32 s56, 16\n"
        "s_cselect_b32 s33, s56, s33\n"
        "s_cselect_b32 s34, 0, 1\n"
        "s_lshl_b32 s56, s3, 3\n"
        "s_add_u32 s56, s56, 3\n"
        "s_cmp_ge_u32 s3, 30\n"
        "s_cselect_b32 s33, s56, s33\n"
        "s_cselect_b32 s37, -1, 1\n"
        "s_mov_b32 s26, 3145728\n"
        "s_mov_b32 s27, s11\n"
        "s_mov_b32 s18, s10\n"
        "s_mov_b32 s19, s11\n"
        "s_mov_b32 s22, s11\n"
        "s_mov_b32 s23, s11\n"
        "v_lshlrev_b32 v2, 4, v1\n"
        "s_lshl_b32 s56, s30, 10\n"
        "s_add_u32 s56, s56, 65536\n"
        "v_add_u32 v140, s56, v2\n"
        "s_sub_u32 s56, s31, s30\n"
        "s_sub_u32 s56, s56, 1\n"
        "s_and_b32 s56, s56, 7\n"
        "s_lshl_b32 s57, s56, 10\n"
        "s_cmp_eq_u32 s56, 7\n"
        "s_cselect_b32 s57, 0x70000000, s57\n"
        "v_add_u32 v141, s57, v2\n"
        "s_add_u32 s29, s31, 1\n"
        "s_and_b32 s29, s29, 7\n"
        "s_lshl_b32 s56, s29, 1\n"
        "s_add_u32 s56, s56, s34\n"
        "s_mul_i32 s48, s56, 36864\n"
        "s_add_u32 s48, s48, 65536\n"
        "v_lshrrev_b32 v9, 3, v1\n"
        "v_and_b32 v10, 7, v1\n"
        "v_mul_u32_u24 v11, 0x700, v9\n"
        "v_lshl_add_u32 v11, v10, 8, v11\n"
        "s_lshl_b32 s56, s34, 7\n"
        "s_add_u32 s56, s56, 4\n"
        "v_add_u32 v11, s56, v11\n"
        "v_cmp_eq_u32 vcc, 7, v10\n"
        "v_bfrev_b32 v12, 14\n"
        "s_mul_i32 s28, s31, 14\n"
        "v_cndmask_b32 v144, v11, v12, vcc\n"
        "s_add_u32 s28, s28, s3\n"
        "s_lshl_b32 s28, s28, 7\n"
        "s_waitcnt lgkmcnt(0)\n"
        "s_lshr_b32 s56, s16, 7\n"
        "s_sub_u32 s33, s33, s56\n"
        "s_and_b32 s33, s33, 15\n"
        "s_lshl_b32 s38, s33, 1\n"
        "s_add_u32 s38, s38, s34\n"
        "s_lshl_b32 s39, s38, 7\n"
        "s_add_u32 s39, s39, 16384\n"
        "s_load_dword s36, s[24:25], s39\n"
        "s_mul_i32 s66, s38, 65536\n"
        "s_add_u32 s66, s66, 1048576\n"
        "s_lshl_b32 s49, s34, 16\n"
        "s_lshl_b32 s56, s33, 7\n"
        "s_add_u32 s49, s49, s56\n"
        "s_cmp_ge_u32 s30, 4\n"
        "s_cbranch_scc1 MATH_%=\n"
        "s_lshl_b32 s57, s31, 13\n"
        "s_lshl_b32 s58, s30, 8\n"
        "s_add_u32 s57, s57, s58\n"
        "s_add_u32 s52, s66, s57\n"
        "s_add_u32 s53, s52, 4096\n"
        "v_lshlrev_b32 v146, 2, v1\n"
        "s_lshl_b32 s57, s31, 10\n"
        "s_add_u32 s57, s57, s58\n"
        "s_add_u32 s57, s57, 65536\n"
        "v_add_u32 v147, s57, v146\n"
        "v_lshrrev_b32 v11, 2, v1\n"
        "v_and_b32 v13, 3, v1\n"
        "v_lshlrev_b32 v13, 1, v13\n"
        "v_lshl_or_b32 v148, v11, 11, v13\n"
        "s_and_b32 s57, s30, 1\n"
        "s_lshl_b32 s57, s57, 15\n"
        "s_add_u32 s49, s49, s57\n"
        "s_lshr_b32 s57, s30, 1\n"
        "s_lshl_b32 s57, s57, 3\n"
        "s_add_u32 s49, s49, s57\n"
        "s_lshl_b32 s57, s31, 4\n"
        "s_add_u32 s49, s49, s57\n"
        "s_lshl_b32 s57, s30, 13\n"
        "s_add_u32 s66, s66, s57\n"
        "v_and_b32 v11, 4, v11\n"
        "v_xor_b32 v11, v11, v10\n"
        "v_lshlrev_b32 v11, 4, v11\n"
        "v_lshl_or_b32 v142, v9, 11, v11\n"
        "v_add_u32 v143, 15360, v142\n"
        "s_mul_i32 s50, s29, 448\n"
        "s_mul_i32 s58, s30, 112\n"
        "s_add_u32 s50, s50, s58\n"
        "s_lshl_b32 s50, s50, 11\n"
        "s_add_u32 s50, s50, s56\n"
        "s_mul_i32 s54, s30, 14336\n"
        "s_lshl_b32 s55, s37, 11\n"
        "s_cmp_lt_u32 s3, 14\n"
        "s_cbranch_scc0 HELPM_%=\n"
        "v_and_b32 v5, 2, v9\n"
        "v_xor_b32 v5, v5, v10\n"
        "v_lshlrev_b32 v5, 4, v5\n"
        "v_mul_u32_u24 v6, 7168, v9\n"
        "v_or_b32 v5, v5, v6\n"
        "v_add_u32 v6, 56320, v5\n"
        "v_xor_b32 v7, 64, v5\n"
        "v_add_u32 v8, 56320, v7\n"
        "s_mul_i32 s63, s31, 7\n"
        "s_lshr_b32 s58, s3, 1\n"
        "s_add_u32 s63, s63, s58\n"
        "s_lshl_b32 s63, s63, 7\n"
        "s_mul_i32 s64, s30, 114688\n"
        "s_add_u32 s63, s63, s64\n"
        "s_lshl_b32 s64, s30, 12\n"
        "s_add_u32 s64, s64, 65536\n"
        "s_mov_b32 s60, s64\n"
        "s_mov_b32 s14, 7342080\n"
        "s_mov_b32 s15, s11\n"
        "s_sub_u32 s12, s12, 2048\n"
        "s_subb_u32 s13, s13, 0\n"
        "s_mov_b32 s58, 458752\n"
        "s_mov_b32 s59, 147456\n"
        "buffer_load_dwordx4 v4, s[4:7], s61 offen lds\n"
        "s_add_u32 m0, m0, s62\n"
        "s_addk_i32 s61, 0x80\n"
        "buffer_load_dwordx4 v4, s[4:7], s61 offen lds\n"
        "s_add_u32 m0, m0, s62\n"
        "s_addk_i32 s61, 0x80\n"
        "buffer_load_dwordx4 v4, s[4:7], s61 offen lds\n"
        "s_add_u32 m0, m0, s62\n"
        "s_addk_i32 s61, 0x80\n"
        "buffer_load_dwordx4 v4, s[4:7], s61 offen lds\n"
        "s_add_u32 m0, m0, s62\n"
        "s_addk_i32 s61, 0x80\n"
        "buffer_load_dwordx4 v4, s[4:7], s61 offen lds\n"
        "s_add_u32 m0, m0, s62\n"
        "s_addk_i32 s61, 0x80\n"
        "buffer_load_dwordx4 v4, s[4:7], s61 offen lds\n"
        "s_add_u32 m0, m0, s62\n"
        "s_addk_i32 s61, 0x80\n"
        "buffer_load_dwordx4 v4, s[4:7], s61 offen lds\n"
        "s_add_u32 m0, m0, s62\n"
        "s_addk_i32 s61, 0x80\n"
        "buffer_load_dwordx4 v4, s[4:7], s61 offen lds\n"
        "s_add_u32 m0, m0, s62\n"
        "s_addk_i32 s61, 0x80\n"
        "s_movk_i32 s65, -4\n"
        "LD_PRO_%=:\n"
        "s_barrier\n"
        "s_mov_b32 m0, s64\n"
        "s_addk_i32 s64, 0x4000\n"
        "buffer_load_dwordx4 v5, s[8:11], s63 offen lds\n"
        "buffer_load_dwordx4 v6, s[8:11], s63 offen offset:1024 lds\n"
        "buffer_load_dwordx4 v7, s[12:15], s63 offen offset:2048 lds\n"
        "buffer_load_dwordx4 v8, s[12:15], s63 offen offset:3072 lds\n"
        "s_cmp_ge_u32 s64, s59\n"
        "s_cselect_b32 s64, s60, s64\n"
        "s_add_u32 s63, s63, s58\n"
        "s_add_u32 s65, s65, 1\n"
        "s_cbranch_scc0 LD_PRO_%=\n"
        "s_movk_i32 s65, -12\n"
        "LD_A_%=:\n"
        "s_waitcnt vmcnt(12)\n"
        "s_barrier\n"
        "s_mov_b32 m0, s64\n"
        "s_addk_i32 s64, 0x4000\n"
        "buffer_load_dwordx4 v5, s[8:11], s63 offen lds\n"
        "buffer_load_dwordx4 v6, s[8:11], s63 offen offset:1024 lds\n"
        "buffer_load_dwordx4 v7, s[12:15], s63 offen offset:2048 lds\n"
        "buffer_load_dwordx4 v8, s[12:15], s63 offen offset:3072 lds\n"
        "s_cmp_ge_u32 s64, s59\n"
        "s_cselect_b32 s64, s60, s64\n"
        "s_add_u32 s63, s63, s58\n"
        "s_add_u32 s65, s65, 1\n"
        "s_cbranch_scc0 LD_A_%=\n"
        "s_mov_b32 m0, s54\n"
        "s_mov_b32 s37, 0x8000\n"
        "s_waitcnt vmcnt(12)\n"
        "s_barrier\n"
        "s_waitcnt vmcnt(8)\n"
        "s_barrier\n"
        "buffer_load_dwordx4 v142, s[16:19], s50 offen lds\n"
        "buffer_load_dwordx4 v143, s[16:19], s50 offen offset:1024 lds\n"
        "s_add_u32 m0, m0, s55\n"
        "s_add_u32 s50, s50, s37\n"
        "buffer_load_dwordx4 v142, s[16:19], s50 offen lds\n"
        "buffer_load_dwordx4 v143, s[16:19], s50 offen offset:1024 lds\n"
        "s_add_u32 m0, m0, s55\n"
        "s_add_u32 s50, s50, s37\n"
        "s_waitcnt vmcnt(8)\n"
        "s_barrier\n"
        "buffer_load_dwordx4 v142, s[16:19], s50 offen lds\n"
        "buffer_load_dwordx4 v143, s[16:19], s50 offen offset:1024 lds\n"
        "s_add_u32 m0, m0, s55\n"
        "s_add_u32 s50, s50, s37\n"
        "buffer_load_dwordx4 v142, s[16:19], s50 offen lds\n"
        "buffer_load_dwordx4 v143, s[16:19], s50 offen offset:1024 lds\n"
        "s_add_u32 m0, m0, s55\n"
        "s_add_u32 s50, s50, s37\n"
        "s_waitcnt vmcnt(8)\n"
        "s_barrier\n"
        "buffer_load_dwordx4 v142, s[16:19], s50 offen lds\n"
        "buffer_load_dwordx4 v143, s[16:19], s50 offen offset:1024 lds\n"
        "s_add_u32 m0, m0, s55\n"
        "s_add_u32 s50, s50, s37\n"
        "buffer_load_dwordx4 v142, s[16:19], s50 offen lds\n"
        "buffer_load_dwordx4 v143, s[16:19], s50 offen offset:1024 lds\n"
        "s_add_u32 m0, m0, s55\n"
        "s_add_u32 s50, s50, s37\n"
        "buffer_load_dwordx4 v142, s[16:19], s50 offen lds\n"
        "buffer_load_dwordx4 v143, s[16:19], s50 offen offset:1024 lds\n"
        "s_add_u32 m0, m0, s55\n"
        "s_add_u32 s50, s50, s37\n"
        "s_branch MEM_END_%=\n"
        "HELPM_%=:\n"
        "s_lshr_b32 s58, s37, 31\n"
        "s_mul_i32 s59, s58, 196608\n"
        "s_add_u32 s50, s50, s59\n"
        "s_mul_i32 s59, s58, 12288\n"
        "s_add_u32 m0, s54, s59\n"
        "s_lshl_b32 s37, s37, 15\n"
        "s_waitcnt vmcnt(4)\n"
        "s_barrier\n"
        "buffer_load_dwordx4 v142, s[16:19], s50 offen lds\n"
        "buffer_load_dwordx4 v143, s[16:19], s50 offen offset:1024 lds\n"
        "s_add_u32 m0, m0, s55\n"
        "s_add_u32 s50, s50, s37\n"
        "buffer_load_dwordx4 v142, s[16:19], s50 offen lds\n"
        "buffer_load_dwordx4 v143, s[16:19], s50 offen offset:1024 lds\n"
        "s_add_u32 m0, m0, s55\n"
        "s_add_u32 s50, s50, s37\n"
        "buffer_load_dwordx4 v142, s[16:19], s50 offen lds\n"
        "buffer_load_dwordx4 v143, s[16:19], s50 offen offset:1024 lds\n"
        "s_add_u32 m0, m0, s55\n"
        "s_add_u32 s50, s50, s37\n"
        "buffer_load_dwordx4 v142, s[16:19], s50 offen lds\n"
        "buffer_load_dwordx4 v143, s[16:19], s50 offen offset:1024 lds\n"
        "s_add_u32 m0, m0, s55\n"
        "s_add_u32 s50, s50, s37\n"
        "buffer_load_dwordx4 v142, s[16:19], s50 offen lds\n"
        "buffer_load_dwordx4 v143, s[16:19], s50 offen offset:1024 lds\n"
        "s_add_u32 m0, m0, s55\n"
        "s_add_u32 s50, s50, s37\n"
        "buffer_load_dwordx4 v142, s[16:19], s50 offen lds\n"
        "buffer_load_dwordx4 v143, s[16:19], s50 offen offset:1024 lds\n"
        "s_add_u32 m0, m0, s55\n"
        "s_add_u32 s50, s50, s37\n"
        "buffer_load_dwordx4 v142, s[16:19], s50 offen lds\n"
        "buffer_load_dwordx4 v143, s[16:19], s50 offen offset:1024 lds\n"
        "s_add_u32 m0, m0, s55\n"
        "s_add_u32 s50, s50, s37\n"
        "MEM_END_%=:\n"
        "s_waitcnt vmcnt(0) lgkmcnt(0)\n"
        "s_cmp_lg_u32 s30, 0\n"
        "s_cbranch_scc1 MEM_W_%=\n"
        "buffer_load_dword v145, v144, s[24:27], 0 offen sc1\n"
        "MEM_W_%=:\n"
        "s_barrier\n"
        "s_add_u32 s33, s36, 1\n"
        "s_and_b32 s57, s33, 0xff\n"
        "s_sub_u32 s60, s68, 130\n"
        "s_movk_i32 s56, 2000\n"
        "s_mov_b32 s62, 32768\n"
        "s_cmp_lg_u32 s30, 0\n"
        "s_cbranch_scc1 RED_%=\n"
        "s_waitcnt vmcnt(0)\n"
        "ds_write_b32 v146, v145 offset:57344\n"
        "s_branch RED_%=\n"
        "MATH_%=:\n"
        "s_cmp_lg_u32 s30, 5\n"
        "s_cbranch_scc1 NOTLB_%=\n"
        "s_cmp_lt_u32 s3, 14\n"
        "s_cbranch_scc1 NOTLB_%=\n"
        "s_add_u32 s56, s66, 7168\n"
        "s_load_dword s69, s[24:25], s56\n"
        "s_mul_i32 s56, s29, 917504\n"
        "s_load_dword s69, s[16:17], s56\n"
        "s_add_u32 s56, s56, 917376\n"
        "s_load_dword s69, s[16:17], s56\n"
        "s_load_dword s69, s[20:21], 0x0\n"
        "NOTLB_%=:\n"
        "s_lshl_b32 s57, s30, 13\n"
        "s_add_u32 s66, s66, s57\n"
        "s_sub_u32 s57, s30, 4\n"
        "s_lshl_b32 s67, %1, 7\n"
        "s_lshl_b32 s56, s57, 5\n"
        "s_add_u32 s67, s67, s56\n"
        "s_add_u32 s67, s67, 32768\n"
        "s_load_dwordx8 s[40:47], s[24:25], s67\n"
        "s_mul_i32 s51, s57, 14336\n"
        "s_mul_i32 s58, s57, 8960\n"
        "s_add_u32 s52, s48, s58\n"
        "s_add_u32 s53, s52, 5120\n"
        "v_lshrrev_b32 v6, 5, v1\n"
        "v_bfe_u32 v4, v1, 2, 2\n"
        "v_bfe_u32 v3, v1, 4, 1\n"
        "v_and_b32 v10, 3, v1\n"
        "v_lshrrev_b32 v9, 1, v4\n"
        "v_lshl_or_b32 v12, v6, 2, v4\n"
        "v_lshlrev_b32 v12, 7, v12\n"
        "v_lshl_or_b32 v12, v3, 5, v12\n"
        "v_lshl_or_b32 v12, v10, 3, v12\n"
        "v_lshl_or_b32 v12, v9, 6, v12\n"
        "v_add_u32 v146, s51, v12\n"
        "v_xor_b32 v147, 64, v146\n"
        "v_lshlrev_b32 v148, 2, v1\n"
        "v_add_u32 v148, 0x400, v148\n"
        "s_lshl_b32 s58, s57, 13\n"
        "s_add_u32 s58, s58, 65536\n"
        "v_add_u32 v149, s58, v2\n"
        "v_sub_u32_dpp v150, v1, v1 wave_ror:1 row_mask:0xf bank_mask:0xf\n"
        "s_mul_i32 s58, s57, 7\n"
        "s_lshr_b32 s58, s58, 2\n"
        "s_min_u32 s58, s58, 4\n"
        "s_lshl_b32 s58, s58, 1\n"
        "s_add_u32 s58, s58, s34\n"
        "s_mul_i32 s56, s29, 14\n"
        "s_add_u32 s58, s58, s56\n"
        "s_lshl_b32 s58, s58, 7\n"
        "s_add_u32 s50, s24, s58\n"
        "s_addc_u32 s51, s25, 0\n"
        "s_lshl_b32 s56, s29, 3\n"
        "s_lshl_b64 s[54:55], 0x7f, s56\n"
        "s_mov_b32 s62, 32768\n"
        "s_cmp_lt_u32 s3, 14\n"
        "s_cbranch_scc1 P1C_%=\n"
        "s_barrier\n"
        "s_sleep 30\n"
        "HM_%=:\n"
        "s_memrealtime s[64:65]\n"
        "s_waitcnt lgkmcnt(0)\n"
        "s_sub_u32 s64, s64, s68\n"
        "s_cmpk_lt_u32 s64, 300\n"
        "s_cbranch_scc1 HM_%=\n"
        "s_load_dword s60, s[50:51], 0x4\n"
        "s_load_dword s61, s[50:51], 0x104\n"
        "s_load_dword s63, s[50:51], 0x204\n"
        "s_branch P2S_%=\n"
        "P1C_%=:\n"
        "s_load_dword s35, s[24:25], s28\n"
        "v_lshl_or_b32 v5, v3, 1, v9\n"
        "v_xor_b32 v5, s57, v5\n"
        "v_lshlrev_b32 v5, 5, v5\n"
        "v_lshl_or_b32 v5, v3, 11, v5\n"
        "v_lshl_or_b32 v5, v6, 10, v5\n"
        "v_lshl_or_b32 v5, v4, 7, v5\n"
        "v_lshl_or_b32 v5, v10, 3, v5\n"
        "v_or_b32 v5, 65536, v5\n"
        "v_add_u32 v11, 0x10000, v5\n"
        "v_and_b32 v3, 31, v1\n"
        "v_bfe_u32 v7, v1, 1, 3\n"
        "v_xor_b32 v7, v7, v6\n"
        "v_lshlrev_b32 v7, 4, v7\n"
        "v_lshl_or_b32 v7, v3, 7, v7\n"
        "v_xor_b32 v8, 32, v7\n"
        "v_xor_b32 v9, 64, v7\n"
        "v_xor_b32 v10, 0x60, v7\n"
        "s_lshl_b32 s59, s31, 1\n"
        "s_and_b32 s56, s3, 1\n"
        "s_add_u32 s59, s59, s56\n"
        "s_mul_i32 s59, s59, 36864\n"
        "s_lshr_b32 s60, s3, 1\n"
        "s_lshl_b32 s60, s60, 2\n"
        "s_add_u32 s60, s60, s57\n"
        "s_mul_i32 s60, s60, 1280\n"
        "s_add_u32 s59, s59, s60\n"
        "s_add_u32 s59, s59, 65536\n"
        "v_mov_b32 v64, s28\n"
        "v_bfrev_b32 v65, 14\n"
        "v_cmp_eq_u32 vcc, 0x100, %2\n"
        "s_nop 1\n"
        "v_cndmask_b32 v64, v65, v64, vcc\n"
        "v_mov_b32 v12, 0xc0135761\n"
        "v_mov_b32 v13, v12\n"
        "v_mov_b32 v14, 0xbdd2d3e8\n"
        "v_mov_b32 v15, v14\n"
        "s_barrier\n"
        "s_barrier\n"
        "s_barrier\n"
        "s_barrier\n"
        "s_waitcnt lgkmcnt(0)\n"
        "s_add_u32 s35, s35, 1\n"
        "v_add_u32 v4, s35, v1\n"
        "v_mov_b32 v66, s35\n"
        "s_barrier\n"
        "ds_read_b128 v[48:51], v7 offset:0\n"
        "ds_read_b128 v[52:55], v8 offset:0\n"
        "ds_read_b64_tr_b16 v[32:33], v5 offset:0\n"
        "ds_read_b64_tr_b16 v[34:35], v5 offset:512\n"
        "ds_read_b64_tr_b16 v[36:37], v5 offset:4096\n"
        "ds_read_b64_tr_b16 v[38:39], v5 offset:4608\n"
        "ds_read_b64_tr_b16 v[40:41], v5 offset:8192\n"
        "ds_read_b64_tr_b16 v[42:43], v5 offset:8704\n"
        "ds_read_b128 v[56:59], v9 offset:0\n"
        "ds_read_b64_tr_b16 v[44:45], v5 offset:12288\n"
        "ds_read_b64_tr_b16 v[46:47], v5 offset:12800\n"
        "ds_read_b128 v[60:63], v10 offset:0\n"
        "s_waitcnt lgkmcnt(8)\n"
        "v_mfma_f32_32x32x16_bf16 v[16:31], v[32:35], v[48:51], 0\n"
        "ds_read_b128 v[48:51], v7 offset:4096\n"
        "s_waitcnt lgkmcnt(7)\n"
        "v_mfma_f32_32x32x16_bf16 v[16:31], v[36:39], v[52:55], v[16:31]\n"
        "ds_read_b128 v[52:55], v8 offset:4096\n"
        "s_waitcnt lgkmcnt(2)\n"
        "s_barrier\n"
        "ds_read_b64_tr_b16 v[32:33], v5 offset:16384\n"
        "ds_read_b64_tr_b16 v[34:35], v5 offset:16896\n"
        "ds_read_b64_tr_b16 v[36:37], v5 offset:20480\n"
        "ds_read_b64_tr_b16 v[38:39], v5 offset:20992\n"
        "v_mfma_f32_32x32x16_bf16 v[16:31], v[40:43], v[56:59], v[16:31]\n"
        "ds_read_b64_tr_b16 v[40:41], v5 offset:24576\n"
        "ds_read_b64_tr_b16 v[42:43], v5 offset:25088\n"
        "ds_read_b128 v[56:59], v9 offset:4096\n"
        "v_mfma_f32_32x32x16_bf16 v[16:31], v[44:47], v[60:63], v[16:31]\n"
        "ds_read_b64_tr_b16 v[44:45], v5 offset:28672\n"
        "ds_read_b64_tr_b16 v[46:47], v5 offset:29184\n"
        "ds_read_b128 v[60:63], v10 offset:4096\n"
        "s_waitcnt lgkmcnt(8)\n"
        "v_mfma_f32_32x32x16_bf16 v[16:31], v[32:35], v[48:51], v[16:31]\n"
        "ds_read_b128 v[48:51], v7 offset:8192\n"
        "s_waitcnt lgkmcnt(7)\n"
        "v_mfma_f32_32x32x16_bf16 v[16:31], v[36:39], v[52:55], v[16:31]\n"
        "ds_read_b128 v[52:55], v8 offset:8192\n"
        "s_waitcnt lgkmcnt(2)\n"
        "s_barrier\n"
        "ds_read_b64_tr_b16 v[32:33], v5 offset:32768\n"
        "ds_read_b64_tr_b16 v[34:35], v5 offset:33280\n"
        "ds_read_b64_tr_b16 v[36:37], v5 offset:36864\n"
        "ds_read_b64_tr_b16 v[38:39], v5 offset:37376\n"
        "v_mfma_f32_32x32x16_bf16 v[16:31], v[40:43], v[56:59], v[16:31]\n"
        "ds_read_b64_tr_b16 v[40:41], v5 offset:40960\n"
        "ds_read_b64_tr_b16 v[42:43], v5 offset:41472\n"
        "ds_read_b128 v[56:59], v9 offset:8192\n"
        "v_mfma_f32_32x32x16_bf16 v[16:31], v[44:47], v[60:63], v[16:31]\n"
        "ds_read_b64_tr_b16 v[44:45], v5 offset:45056\n"
        "ds_read_b64_tr_b16 v[46:47], v5 offset:45568\n"
        "ds_read_b128 v[60:63], v10 offset:8192\n"
        "s_waitcnt lgkmcnt(8)\n"
        "v_mfma_f32_32x32x16_bf16 v[16:31], v[32:35], v[48:51], v[16:31]\n"
        "ds_read_b128 v[48:51], v7 offset:12288\n"
        "s_waitcnt lgkmcnt(7)\n"
        "v_mfma_f32_32x32x16_bf16 v[16:31], v[36:39], v[52:55], v[16:31]\n"
        "ds_read_b128 v[52:55], v8 offset:12288\n"
        "s_waitcnt lgkmcnt(2)\n"
        "s_barrier\n"
        "ds_read_b64_tr_b16 v[32:33], v5 offset:49152\n"
        "ds_read_b64_tr_b16 v[34:35], v5 offset:49664\n"
        "ds_read_b64_tr_b16 v[36:37], v5 offset:53248\n"
        "ds_read_b64_tr_b16 v[38:39], v5 offset:53760\n"
        "v_mfma_f32_32x32x16_bf16 v[16:31], v[40:43], v[56:59], v[16:31]\n"
        "ds_read_b64_tr_b16 v[40:41], v5 offset:57344\n"
        "ds_read_b64_tr_b16 v[42:43], v5 offset:57856\n"
        "ds_read_b128 v[56:59], v9 offset:12288\n"
        "v_mfma_f32_32x32x16_bf16 v[16:31], v[44:47], v[60:63], v[16:31]\n"
        "ds_read_b64_tr_b16 v[44:45], v5 offset:61440\n"
        "ds_read_b64_tr_b16 v[46:47], v5 offset:61952\n"
        "ds_read_b128 v[60:63], v10 offset:12288\n"
        "s_waitcnt lgkmcnt(8)\n"
        "v_mfma_f32_32x32x16_bf16 v[16:31], v[32:35], v[48:51], v[16:31]\n"
        "ds_read_b128 v[48:51], v7 offset:16384\n"
        "s_waitcnt lgkmcnt(7)\n"
        "v_mfma_f32_32x32x16_bf16 v[16:31], v[36:39], v[52:55], v[16:31]\n"
        "ds_read_b128 v[52:55], v8 offset:16384\n"
        "s_waitcnt lgkmcnt(2)\n"
        "s_barrier\n"
        "ds_read_b64_tr_b16 v[32:33], v11 offset:0\n"
        "ds_read_b64_tr_b16 v[34:35], v11 offset:512\n"
        "ds_read_b64_tr_b16 v[36:37], v11 offset:4096\n"
        "ds_read_b64_tr_b16 v[38:39], v11 offset:4608\n"
        "v_mfma_f32_32x32x16_bf16 v[16:31], v[40:43], v[56:59], v[16:31]\n"
        "ds_read_b64_tr_b16 v[40:41], v11 offset:8192\n"
        "ds_read_b64_tr_b16 v[42:43], v11 offset:8704\n"
        "ds_read_b128 v[56:59], v9 offset:16384\n"
        "v_mfma_f32_32x32x16_bf16 v[16:31], v[44:47], v[60:63], v[16:31]\n"
        "ds_read_b64_tr_b16 v[44:45], v11 offset:12288\n"
        "ds_read_b64_tr_b16 v[46:47], v11 offset:12800\n"
        "ds_read_b128 v[60:63], v10 offset:16384\n"
        "s_waitcnt lgkmcnt(8)\n"
        "v_mfma_f32_32x32x16_bf16 v[16:31], v[32:35], v[48:51], v[16:31]\n"
        "ds_read_b128 v[48:51], v7 offset:20480\n"
        "s_waitcnt lgkmcnt(7)\n"
        "v_mfma_f32_32x32x16_bf16 v[16:31], v[36:39], v[52:55], v[16:31]\n"
        "ds_read_b128 v[52:55], v8 offset:20480\n"
        "s_waitcnt lgkmcnt(2)\n"
        "s_barrier\n"
        "s_memrealtime s[56:57]\n"
        "s_waitcnt lgkmcnt(0)\n"
        "s_addk_i32 s56, 190\n"
        "v_mov_b32 v67, s56\n"
        "s_setprio 3\n"
        "buffer_store_dwordx2 v[66:67], v64, s[24:27], 0 offen sc1\n"
        "s_setprio 0\n"
        "ds_read_b64_tr_b16 v[32:33], v5 offset:0\n"
        "ds_read_b64_tr_b16 v[34:35], v5 offset:512\n"
        "ds_read_b64_tr_b16 v[36:37], v5 offset:4096\n"
        "ds_read_b64_tr_b16 v[38:39], v5 offset:4608\n"
        "v_mfma_f32_32x32x16_bf16 v[16:31], v[40:43], v[56:59], v[16:31]\n"
        "ds_read_b64_tr_b16 v[40:41], v5 offset:8192\n"
        "ds_read_b64_tr_b16 v[42:43], v5 offset:8704\n"
        "ds_read_b128 v[56:59], v9 offset:20480\n"
        "v_mfma_f32_32x32x16_bf16 v[16:31], v[44:47], v[60:63], v[16:31]\n"
        "ds_read_b64_tr_b16 v[44:45], v5 offset:12288\n"
        "ds_read_b64_tr_b16 v[46:47], v5 offset:12800\n"
        "ds_read_b128 v[60:63], v10 offset:20480\n"
        "s_waitcnt lgkmcnt(8)\n"
        "v_mfma_f32_32x32x16_bf16 v[16:31], v[32:35], v[48:51], v[16:31]\n"
        "ds_read_b128 v[48:51], v7 offset:24576\n"
        "s_waitcnt lgkmcnt(7)\n"
        "v_mfma_f32_32x32x16_bf16 v[16:31], v[36:39], v[52:55], v[16:31]\n"
        "ds_read_b128 v[52:55], v8 offset:24576\n"
        "s_waitcnt lgkmcnt(2)\n"
        "s_barrier\n"
        "ds_read_b64_tr_b16 v[32:33], v5 offset:16384\n"
        "ds_read_b64_tr_b16 v[34:35], v5 offset:16896\n"
        "ds_read_b64_tr_b16 v[36:37], v5 offset:20480\n"
        "ds_read_b64_tr_b16 v[38:39], v5 offset:20992\n"
        "v_mfma_f32_32x32x16_bf16 v[16:31], v[40:43], v[56:59], v[16:31]\n"
        "ds_read_b64_tr_b16 v[40:41], v5 offset:24576\n"
        "ds_read_b64_tr_b16 v[42:43], v5 offset:25088\n"
        "ds_read_b128 v[56:59], v9 offset:24576\n"
        "v_mfma_f32_32x32x16_bf16 v[16:31], v[44:47], v[60:63], v[16:31]\n"
        "ds_read_b64_tr_b16 v[44:45], v5 offset:28672\n"
        "ds_read_b64_tr_b16 v[46:47], v5 offset:29184\n"
        "ds_read_b128 v[60:63], v10 offset:24576\n"
        "s_waitcnt lgkmcnt(8)\n"
        "v_mfma_f32_32x32x16_bf16 v[16:31], v[32:35], v[48:51], v[16:31]\n"
        "ds_read_b128 v[48:51], v7 offset:28672\n"
        "s_waitcnt lgkmcnt(7)\n"
        "v_mfma_f32_32x32x16_bf16 v[16:31], v[36:39], v[52:55], v[16:31]\n"
        "ds_read_b128 v[52:55], v8 offset:28672\n"
        "s_waitcnt lgkmcnt(2)\n"
        "s_barrier\n"
        "ds_read_b64_tr_b16 v[32:33], v5 offset:32768\n"
        "ds_read_b64_tr_b16 v[34:35], v5 offset:33280\n"
        "ds_read_b64_tr_b16 v[36:37], v5 offset:36864\n"
        "ds_read_b64_tr_b16 v[38:39], v5 offset:37376\n"
        "v_mfma_f32_32x32x16_bf16 v[16:31], v[40:43], v[56:59], v[16:31]\n"
        "ds_read_b64_tr_b16 v[40:41], v5 offset:40960\n"
        "ds_read_b64_tr_b16 v[42:43], v5 offset:41472\n"
        "ds_read_b128 v[56:59], v9 offset:28672\n"
        "v_mfma_f32_32x32x16_bf16 v[16:31], v[44:47], v[60:63], v[16:31]\n"
        "ds_read_b64_tr_b16 v[44:45], v5 offset:45056\n"
        "ds_read_b64_tr_b16 v[46:47], v5 offset:45568\n"
        "ds_read_b128 v[60:63], v10 offset:28672\n"
        "s_waitcnt lgkmcnt(8)\n"
        "v_mfma_f32_32x32x16_bf16 v[16:31], v[32:35], v[48:51], v[16:31]\n"
        "ds_read_b128 v[48:51], v7 offset:32768\n"
        "s_waitcnt lgkmcnt(7)\n"
        "v_mfma_f32_32x32x16_bf16 v[16:31], v[36:39], v[52:55], v[16:31]\n"
        "ds_read_b128 v[52:55], v8 offset:32768\n"
        "s_waitcnt lgkmcnt(2)\n"
        "s_barrier\n"
        "ds_read_b64_tr_b16 v[32:33], v5 offset:49152\n"
        "ds_read_b64_tr_b16 v[34:35], v5 offset:49664\n"
        "ds_read_b64_tr_b16 v[36:37], v5 offset:53248\n"
        "ds_read_b64_tr_b16 v[38:39], v5 offset:53760\n"
        "v_mfma_f32_32x32x16_bf16 v[16:31], v[40:43], v[56:59], v[16:31]\n"
        "ds_read_b64_tr_b16 v[40:41], v5 offset:57344\n"
        "ds_read_b64_tr_b16 v[42:43], v5 offset:57856\n"
        "ds_read_b128 v[56:59], v9 offset:32768\n"
        "v_mfma_f32_32x32x16_bf16 v[16:31], v[44:47], v[60:63], v[16:31]\n"
        "ds_read_b64_tr_b16 v[44:45], v5 offset:61440\n"
        "ds_read_b64_tr_b16 v[46:47], v5 offset:61952\n"
        "ds_read_b128 v[60:63], v10 offset:32768\n"
        "s_waitcnt lgkmcnt(8)\n"
        "v_mfma_f32_32x32x16_bf16 v[16:31], v[32:35], v[48:51], v[16:31]\n"
        "ds_read_b128 v[48:51], v7 offset:36864\n"
        "s_waitcnt lgkmcnt(7)\n"
        "v_mfma_f32_32x32x16_bf16 v[16:31], v[36:39], v[52:55], v[16:31]\n"
        "ds_read_b128 v[52:55], v8 offset:36864\n"
        "s_waitcnt lgkmcnt(2)\n"
        "s_barrier\n"
        "ds_read_b64_tr_b16 v[32:33], v11 offset:0\n"
        "ds_read_b64_tr_b16 v[34:35], v11 offset:512\n"
        "ds_read_b64_tr_b16 v[36:37], v11 offset:4096\n"
        "ds_read_b64_tr_b16 v[38:39], v11 offset:4608\n"
        "v_mfma_f32_32x32x16_bf16 v[16:31], v[40:43], v[56:59], v[16:31]\n"
        "ds_read_b64_tr_b16 v[40:41], v11 offset:8192\n"
        "ds_read_b64_tr_b16 v[42:43], v11 offset:8704\n"
        "ds_read_b128 v[56:59], v9 offset:36864\n"
        "v_mfma_f32_32x32x16_bf16 v[16:31], v[44:47], v[60:63], v[16:31]\n"
        "ds_read_b64_tr_b16 v[44:45], v11 offset:12288\n"
        "ds_read_b64_tr_b16 v[46:47], v11 offset:12800\n"
        "ds_read_b128 v[60:63], v10 offset:36864\n"
        "s_waitcnt lgkmcnt(8)\n"
        "v_mfma_f32_32x32x16_bf16 v[16:31], v[32:35], v[48:51], v[16:31]\n"
        "ds_read_b128 v[48:51], v7 offset:40960\n"
        "s_waitcnt lgkmcnt(7)\n"
        "v_mfma_f32_32x32x16_bf16 v[16:31], v[36:39], v[52:55], v[16:31]\n"
        "ds_read_b128 v[52:55], v8 offset:40960\n"
        "s_waitcnt lgkmcnt(2)\n"
        "s_barrier\n"
        "ds_read_b64_tr_b16 v[32:33], v5 offset:0\n"
        "ds_read_b64_tr_b16 v[34:35], v5 offset:512\n"
        "ds_read_b64_tr_b16 v[36:37], v5 offset:4096\n"
        "ds_read_b64_tr_b16 v[38:39], v5 offset:4608\n"
        "v_mfma_f32_32x32x16_bf16 v[16:31], v[40:43], v[56:59], v[16:31]\n"
        "ds_read_b64_tr_b16 v[40:41], v5 offset:8192\n"
        "ds_read_b64_tr_b16 v[42:43], v5 offset:8704\n"
        "ds_read_b128 v[56:59], v9 offset:40960\n"
        "v_mfma_f32_32x32x16_bf16 v[16:31], v[44:47], v[60:63], v[16:31]\n"
        "ds_read_b64_tr_b16 v[44:45], v5 offset:12288\n"
        "ds_read_b64_tr_b16 v[46:47], v5 offset:12800\n"
        "ds_read_b128 v[60:63], v10 offset:40960\n"
        "s_waitcnt lgkmcnt(8)\n"
        "v_mfma_f32_32x32x16_bf16 v[16:31], v[32:35], v[48:51], v[16:31]\n"
        "ds_read_b128 v[48:51], v7 offset:45056\n"
        "s_waitcnt lgkmcnt(7)\n"
        "v_mfma_f32_32x32x16_bf16 v[16:31], v[36:39], v[52:55], v[16:31]\n"
        "ds_read_b128 v[52:55], v8 offset:45056\n"
        "s_waitcnt lgkmcnt(2)\n"
        "s_barrier\n"
        "ds_read_b64_tr_b16 v[32:33], v5 offset:16384\n"
        "ds_read_b64_tr_b16 v[34:35], v5 offset:16896\n"
        "ds_read_b64_tr_b16 v[36:37], v5 offset:20480\n"
        "ds_read_b64_tr_b16 v[38:39], v5 offset:20992\n"
        "v_mfma_f32_32x32x16_bf16 v[16:31], v[40:43], v[56:59], v[16:31]\n"
        "ds_read_b64_tr_b16 v[40:41], v5 offset:24576\n"
        "ds_read_b64_tr_b16 v[42:43], v5 offset:25088\n"
        "ds_read_b128 v[56:59], v9 offset:45056\n"
        "v_mfma_f32_32x32x16_bf16 v[16:31], v[44:47], v[60:63], v[16:31]\n"
        "ds_read_b64_tr_b16 v[44:45], v5 offset:28672\n"
        "ds_read_b64_tr_b16 v[46:47], v5 offset:29184\n"
        "ds_read_b128 v[60:63], v10 offset:45056\n"
        "s_waitcnt lgkmcnt(8)\n"
        "v_mfma_f32_32x32x16_bf16 v[16:31], v[32:35], v[48:51], v[16:31]\n"
        "ds_read_b128 v[48:51], v7 offset:49152\n"
        "s_waitcnt lgkmcnt(7)\n"
        "v_mfma_f32_32x32x16_bf16 v[16:31], v[36:39], v[52:55], v[16:31]\n"
        "ds_read_b128 v[52:55], v8 offset:49152\n"
        "s_waitcnt lgkmcnt(2)\n"
        "s_barrier\n"
        "ds_read_b64_tr_b16 v[32:33], v5 offset:32768\n"
        "ds_read_b64_tr_b16 v[34:35], v5 offset:33280\n"
        "ds_read_b64_tr_b16 v[36:37], v5 offset:36864\n"
        "ds_read_b64_tr_b16 v[38:39], v5 offset:37376\n"
        "v_mfma_f32_32x32x16_bf16 v[16:31], v[40:43], v[56:59], v[16:31]\n"
        "ds_read_b64_tr_b16 v[40:41], v5 offset:40960\n"
        "ds_read_b64_tr_b16 v[42:43], v5 offset:41472\n"
        "ds_read_b128 v[56:59], v9 offset:49152\n"
        "v_mfma_f32_32x32x16_bf16 v[16:31], v[44:47], v[60:63], v[16:31]\n"
        "ds_read_b64_tr_b16 v[44:45], v5 offset:45056\n"
        "ds_read_b64_tr_b16 v[46:47], v5 offset:45568\n"
        "ds_read_b128 v[60:63], v10 offset:49152\n"
        "s_waitcnt lgkmcnt(8)\n"
        "v_mfma_f32_32x32x16_bf16 v[16:31], v[32:35], v[48:51], v[16:31]\n"
        "ds_read_b128 v[48:51], v7 offset:53248\n"
        "s_waitcnt lgkmcnt(7)\n"
        "v_mfma_f32_32x32x16_bf16 v[16:31], v[36:39], v[52:55], v[16:31]\n"
        "ds_read_b128 v[52:55], v8 offset:53248\n"
        "s_waitcnt lgkmcnt(2)\n"
        "s_barrier\n"
        "ds_read_b64_tr_b16 v[32:33], v5 offset:49152\n"
        "ds_read_b64_tr_b16 v[34:35], v5 offset:49664\n"
        "ds_read_b64_tr_b16 v[36:37], v5 offset:53248\n"
        "ds_read_b64_tr_b16 v[38:39], v5 offset:53760\n"
        "v_mfma_f32_32x32x16_bf16 v[16:31], v[40:43], v[56:59], v[16:31]\n"
        "ds_read_b64_tr_b16 v[40:41], v5 offset:57344\n"
        "ds_read_b64_tr_b16 v[42:43], v5 offset:57856\n"
        "ds_read_b128 v[56:59], v9 offset:53248\n"
        "v_mfma_f32_32x32x16_bf16 v[16:31], v[44:47], v[60:63], v[16:31]\n"
        "ds_read_b64_tr_b16 v[44:45], v5 offset:61440\n"
        "ds_read_b64_tr_b16 v[46:47], v5 offset:61952\n"
        "ds_read_b128 v[60:63], v10 offset:53248\n"
        "s_waitcnt lgkmcnt(8)\n"
        "v_mfma_f32_32x32x16_bf16 v[16:31], v[32:35], v[48:51], v[16:31]\n"
        "ds_read_b128 v[48:51], v7 offset:57344\n"
        "s_waitcnt lgkmcnt(7)\n"
        "v_mfma_f32_32x32x16_bf16 v[16:31], v[36:39], v[52:55], v[16:31]\n"
        "ds_read_b128 v[52:55], v8 offset:57344\n"
        "s_waitcnt lgkmcnt(2)\n"
        "s_barrier\n"
        "ds_read_b64_tr_b16 v[32:33], v11 offset:0\n"
        "ds_read_b64_tr_b16 v[34:35], v11 offset:512\n"
        "ds_read_b64_tr_b16 v[36:37], v11 offset:4096\n"
        "ds_read_b64_tr_b16 v[38:39], v11 offset:4608\n"
        "v_mfma_f32_32x32x16_bf16 v[16:31], v[40:43], v[56:59], v[16:31]\n"
        "ds_read_b64_tr_b16 v[40:41], v11 offset:8192\n"
        "ds_read_b64_tr_b16 v[42:43], v11 offset:8704\n"
        "ds_read_b128 v[56:59], v9 offset:57344\n"
        "v_mfma_f32_32x32x16_bf16 v[16:31], v[44:47], v[60:63], v[16:31]\n"
        "ds_read_b64_tr_b16 v[44:45], v11 offset:12288\n"
        "ds_read_b64_tr_b16 v[46:47], v11 offset:12800\n"
        "ds_read_b128 v[60:63], v10 offset:57344\n"
        "s_waitcnt lgkmcnt(8)\n"
        "v_mfma_f32_32x32x16_bf16 v[16:31], v[32:35], v[48:51], v[16:31]\n"
        "ds_read_b128 v[48:51], v7 offset:61440\n"
        "s_waitcnt lgkmcnt(7)\n"
        "v_mfma_f32_32x32x16_bf16 v[16:31], v[36:39], v[52:55], v[16:31]\n"
        "ds_read_b128 v[52:55], v8 offset:61440\n"
        "s_waitcnt lgkmcnt(2)\n"
        "s_barrier\n"
        "ds_read_b64_tr_b16 v[32:33], v5 offset:0\n"
        "ds_read_b64_tr_b16 v[34:35], v5 offset:512\n"
        "ds_read_b64_tr_b16 v[36:37], v5 offset:4096\n"
        "ds_read_b64_tr_b16 v[38:39], v5 offset:4608\n"
        "v_mfma_f32_32x32x16_bf16 v[16:31], v[40:43], v[56:59], v[16:31]\n"
        "ds_read_b64_tr_b16 v[40:41], v5 offset:8192\n"
        "ds_read_b64_tr_b16 v[42:43], v5 offset:8704\n"
        "ds_read_b128 v[56:59], v9 offset:61440\n"
        "v_mfma_f32_32x32x16_bf16 v[16:31], v[44:47], v[60:63], v[16:31]\n"
        "ds_read_b64_tr_b16 v[44:45], v5 offset:12288\n"
        "ds_read_b64_tr_b16 v[46:47], v5 offset:12800\n"
        "ds_read_b128 v[60:63], v10 offset:61440\n"
        "s_waitcnt lgkmcnt(8)\n"
        "v_mfma_f32_32x32x16_bf16 v[16:31], v[32:35], v[48:51], v[16:31]\n"
        "s_waitcnt lgkmcnt(6)\n"
        "v_mfma_f32_32x32x16_bf16 v[16:31], v[36:39], v[52:55], v[16:31]\n"
        "s_waitcnt lgkmcnt(3)\n"
        "v_mfma_f32_32x32x16_bf16 v[16:31], v[40:43], v[56:59], v[16:31]\n"
        "s_waitcnt lgkmcnt(0)\n"
        "v_mfma_f32_32x32x16_bf16 v[16:31], v[44:47], v[60:63], v[16:31]\n"
        "s_load_dword s60, s[50:51], 0x4\n"
        "s_load_dword s61, s[50:51], 0x104\n"
        "s_load_dword s63, s[50:51], 0x204\n"
        "s_nop 8\n"
        "v_pk_mul_f32 v[32:33], v[16:17], v[16:17]\n"
        "v_pk_mul_f32 v[34:35], v[18:19], v[18:19]\n"
        "v_pk_mul_f32 v[36:37], v[20:21], v[20:21]\n"
        "v_pk_mul_f32 v[38:39], v[22:23], v[22:23]\n"
        "v_pk_fma_f32 v[32:33], v[32:33], v[14:15], v[12:13]\n"
        "v_pk_fma_f32 v[34:35], v[34:35], v[14:15], v[12:13]\n"
        "v_pk_fma_f32 v[36:37], v[36:37], v[14:15], v[12:13]\n"
        "v_pk_fma_f32 v[38:39], v[38:39], v[14:15], v[12:13]\n"
        "v_pk_mul_f32 v[32:33], v[32:33], v[16:17]\n"
        "v_pk_mul_f32 v[34:35], v[34:35], v[18:19]\n"
        "v_pk_mul_f32 v[36:37], v[36:37], v[20:21]\n"
        "v_pk_mul_f32 v[38:39], v[38:39], v[22:23]\n"
        "v_exp_f32 v32, v32\n"
        "v_exp_f32 v33, v33\n"
        "v_exp_f32 v34, v34\n"
        "v_exp_f32 v35, v35\n"
        "v_exp_f32 v36, v36\n"
        "v_exp_f32 v37, v37\n"
        "v_exp_f32 v38, v38\n"
        "v_exp_f32 v39, v39\n"
        "v_pk_add_f32 v[32:33], v[32:33], 1.0 op_sel_hi:[1,0]\n"
        "v_pk_add_f32 v[34:35], v[34:35], 1.0 op_sel_hi:[1,0]\n"
        "v_pk_add_f32 v[36:37], v[36:37], 1.0 op_sel_hi:[1,0]\n"
        "v_pk_add_f32 v[38:39], v[38:39], 1.0 op_sel_hi:[1,0]\n"
        "v_rcp_f32 v32, v32\n"
        "v_rcp_f32 v33, v33\n"
        "v_rcp_f32 v34, v34\n"
        "v_rcp_f32 v35, v35\n"
        "v_rcp_f32 v36, v36\n"
        "v_rcp_f32 v37, v37\n"
        "v_rcp_f32 v38, v38\n"
        "v_rcp_f32 v39, v39\n"
        "v_pk_mul_f32 v[32:33], v[32:33], v[16:17]\n"
        "v_pk_mul_f32 v[34:35], v[34:35], v[18:19]\n"
        "v_pk_mul_f32 v[36:37], v[36:37], v[20:21]\n"
        "v_pk_mul_f32 v[38:39], v[38:39], v[22:23]\n"
        "v_pk_mul_f32 v[32:33], v[32:33], v[24:25]\n"
        "v_pk_mul_f32 v[34:35], v[34:35], v[26:27]\n"
        "v_pk_mul_f32 v[36:37], v[36:37], v[28:29]\n"
        "v_pk_mul_f32 v[38:39], v[38:39], v[30:31]\n"
        "v_cvt_pk_bf16_f32 v40, v32, v33\n"
        "v_cvt_pk_bf16_f32 v41, v34, v35\n"
        "v_cvt_pk_bf16_f32 v42, v36, v37\n"
        "v_cvt_pk_bf16_f32 v43, v38, v39\n"
        "buffer_store_dwordx4 v[40:43], v2, s[24:27], s59 offen sc1\n"
        "v_add3_u32 v6, v40, v41, v42\n"
        "v_sub_u32 v4, v4, v43\n"
        "v_sub_u32 v4, v4, v6\n"
        "buffer_store_dword v4, v148, s[24:27], s59 offen sc1\n"
        "P2S_%=:\n"
        "s_waitcnt lgkmcnt(0)\n"
        "s_sub_u32 s60, s60, s68\n"
        "s_sub_u32 s61, s61, s68\n"
        "s_sub_u32 s63, s63, s68\n"
        "s_max_u32 s60, s60, s61\n"
        "s_max_u32 s60, s60, s63\n"
        "s_cmpk_ge_u32 s60, 2000\n"
        "s_cbranch_scc1 P2_%=\n"
        "P2S_TW_%=:\n"
        "s_memrealtime s[64:65]\n"
        "s_waitcnt lgkmcnt(0)\n"
        "s_sub_u32 s64, s64, s68\n"
        "s_cmp_lt_u32 s64, s60\n"
        "s_cbranch_scc1 P2S_TW_%=\n"
        "P2_GO_%=:\n"
        "buffer_load_dwordx4 v[104:107], v2, s[24:27], s52 offen sc1\n"
        "buffer_load_dword v132, v148, s[24:27], s52 offen sc1\n"
        "buffer_load_dwordx4 v[108:111], v2, s[24:27], s52 offen offset:1280 sc1\n"
        "buffer_load_dword v133, v148, s[24:27], s52 offen offset:1280 sc1\n"
        "buffer_load_dwordx4 v[112:115], v2, s[24:27], s52 offen offset:2560 sc1\n"
        "buffer_load_dword v134, v148, s[24:27], s52 offen offset:2560 sc1\n"
        "buffer_load_dwordx4 v[116:119], v2, s[24:27], s52 offen offset:3840 sc1\n"
        "buffer_load_dword v135, v148, s[24:27], s52 offen offset:3840 sc1\n"
        "buffer_load_dwordx4 v[120:123], v2, s[24:27], s53 offen sc1\n"
        "buffer_load_dword v136, v148, s[24:27], s53 offen sc1\n"
        "buffer_load_dwordx4 v[124:127], v2, s[24:27], s53 offen offset:1280 sc1\n"
        "buffer_load_dword v137, v148, s[24:27], s53 offen offset:1280 sc1\n"
        "buffer_load_dwordx4 v[128:131], v2, s[24:27], s53 offen offset:2560 sc1\n"
        "buffer_load_dword v138, v148, s[24:27], s53 offen offset:2560 sc1\n"
        "s_waitcnt lgkmcnt(0)\n"
        "s_barrier\n"
        "ds_read_b64_tr_b16 v[48:49], v146 offset:0\n"
        "ds_read_b64_tr_b16 v[50:51], v146 offset:1024\n"
        "ds_read_b64_tr_b16 v[52:53], v147 offset:0\n"
        "ds_read_b64_tr_b16 v[54:55], v147 offset:1024\n"
        "ds_read_b64_tr_b16 v[56:57], v146 offset:2048\n"
        "ds_read_b64_tr_b16 v[58:59], v146 offset:3072\n"
        "ds_read_b64_tr_b16 v[60:61], v147 offset:2048\n"
        "ds_read_b64_tr_b16 v[62:63], v147 offset:3072\n"
        "ds_read_b64_tr_b16 v[64:65], v146 offset:4096\n"
        "ds_read_b64_tr_b16 v[66:67], v146 offset:5120\n"
        "ds_read_b64_tr_b16 v[68:69], v147 offset:4096\n"
        "ds_read_b64_tr_b16 v[70:71], v147 offset:5120\n"
        "ds_read_b64_tr_b16 v[72:73], v146 offset:6144\n"
        "ds_read_b64_tr_b16 v[74:75], v146 offset:7168\n"
        "ds_read_b64_tr_b16 v[76:77], v147 offset:6144\n"
        "ds_read_b64_tr_b16 v[78:79], v147 offset:7168\n"
        "ds_read_b64_tr_b16 v[80:81], v146 offset:8192\n"
        "ds_read_b64_tr_b16 v[82:83], v146 offset:9216\n"
        "ds_read_b64_tr_b16 v[84:85], v147 offset:8192\n"
        "ds_read_b64_tr_b16 v[86:87], v147 offset:9216\n"
        "ds_read_b64_tr_b16 v[88:89], v146 offset:10240\n"
        "ds_read_b64_tr_b16 v[90:91], v146 offset:11264\n"
        "ds_read_b64_tr_b16 v[92:93], v147 offset:10240\n"
        "ds_read_b64_tr_b16 v[94:95], v147 offset:11264\n"
        "ds_read_b64_tr_b16 v[96:97], v146 offset:12288\n"
        "ds_read_b64_tr_b16 v[98:99], v146 offset:13312\n"
        "ds_read_b64_tr_b16 v[100:101], v147 offset:12288\n"
        "ds_read_b64_tr_b16 v[102:103], v147 offset:13312\n"
        "P2_TRY_%=:\n"
        "s_waitcnt vmcnt(12) lgkmcnt(15)\n"
        "v_mfma_f32_32x32x16_bf16 v[16:31], v[48:51], v[104:107], 0\n"
        "v_add3_u32 v11, v104, v105, v106\n"
        "v_add3_u32 v132, v11, v107, v132\n"
        "v_xor_b32 v13, s40, v132\n"
        "v_mfma_f32_32x32x16_bf16 v[32:47], v[52:55], v[104:107], 0\n"
        "v_sub_u32_dpp v12, v132, v132 wave_ror:1 row_mask:0xf bank_mask:0xf\n"
        "v_xor_b32 v14, v12, v150\n"
        "s_waitcnt vmcnt(10) lgkmcnt(15)\n"
        "v_mfma_f32_32x32x16_bf16 v[16:31], v[56:59], v[108:111], v[16:31]\n"
        "v_add3_u32 v11, v108, v109, v110\n"
        "v_add3_u32 v133, v11, v111, v133\n"
        "v_xor_b32 v11, s41, v133\n"
        "v_mfma_f32_32x32x16_bf16 v[32:47], v[60:63], v[108:111], v[32:47]\n"
        "v_sub_u32_dpp v12, v133, v133 wave_ror:1 row_mask:0xf bank_mask:0xf\n"
        "v_min_u32 v13, v13, v11\n"
        "v_bitop3_b32 v14, v14, v12, v150 bitop3:0xf6\n"
        "s_waitcnt vmcnt(8) lgkmcnt(15)\n"
        "v_mfma_f32_32x32x16_bf16 v[16:31], v[64:67], v[112:115], v[16:31]\n"
        "v_add3_u32 v11, v112, v113, v114\n"
        "v_add3_u32 v134, v11, v115, v134\n"
        "v_xor_b32 v11, s42, v134\n"
        "v_mfma_f32_32x32x16_bf16 v[32:47], v[68:71], v[112:115], v[32:47]\n"
        "v_sub_u32_dpp v12, v134, v134 wave_ror:1 row_mask:0xf bank_mask:0xf\n"
        "v_min_u32 v13, v13, v11\n"
        "v_bitop3_b32 v14, v14, v12, v150 bitop3:0xf6\n"
        "s_waitcnt vmcnt(6) lgkmcnt(12)\n"
        "v_mfma_f32_32x32x16_bf16 v[16:31], v[72:75], v[116:119], v[16:31]\n"
        "v_add3_u32 v11, v116, v117, v118\n"
        "v_add3_u32 v135, v11, v119, v135\n"
        "v_xor_b32 v11, s43, v135\n"
        "v_mfma_f32_32x32x16_bf16 v[32:47], v[76:79], v[116:119], v[32:47]\n"
        "v_sub_u32_dpp v12, v135, v135 wave_ror:1 row_mask:0xf bank_mask:0xf\n"
        "v_min_u32 v13, v13, v11\n"
        "v_bitop3_b32 v14, v14, v12, v150 bitop3:0xf6\n"
        "s_waitcnt vmcnt(4) lgkmcnt(8)\n"
        "v_mfma_f32_32x32x16_bf16 v[16:31], v[80:83], v[120:123], v[16:31]\n"
        "v_add3_u32 v11, v120, v121, v122\n"
        "v_add3_u32 v136, v11, v123, v136\n"
        "v_xor_b32 v11, s44, v136\n"
        "v_mfma_f32_32x32x16_bf16 v[32:47], v[84:87], v[120:123], v[32:47]\n"
        "v_sub_u32_dpp v12, v136, v136 wave_ror:1 row_mask:0xf bank_mask:0xf\n"
        "v_min_u32 v13, v13, v11\n"
        "v_bitop3_b32 v14, v14, v12, v150 bitop3:0xf6\n"
        "s_waitcnt vmcnt(2) lgkmcnt(4)\n"
        "v_mfma_f32_32x32x16_bf16 v[16:31], v[88:91], v[124:127], v[16:31]\n"
        "v_add3_u32 v11, v124, v125, v126\n"
        "v_add3_u32 v137, v11, v127, v137\n"
        "v_xor_b32 v11, s45, v137\n"
        "v_mfma_f32_32x32x16_bf16 v[32:47], v[92:95], v[124:127], v[32:47]\n"
        "v_sub_u32_dpp v12, v137, v137 wave_ror:1 row_mask:0xf bank_mask:0xf\n"
        "v_min_u32 v13, v13, v11\n"
        "v_bitop3_b32 v14, v14, v12, v150 bitop3:0xf6\n"
        "s_waitcnt vmcnt(0) lgkmcnt(0)\n"
        "v_mfma_f32_32x32x16_bf16 v[16:31], v[96:99], v[128:131], v[16:31]\n"
        "v_add3_u32 v11, v128, v129, v130\n"
        "v_add3_u32 v138, v11, v131, v138\n"
        "v_xor_b32 v11, s46, v138\n"
        "v_mfma_f32_32x32x16_bf16 v[32:47], v[100:103], v[128:131], v[32:47]\n"
        "v_sub_u32_dpp v12, v138, v138 wave_ror:1 row_mask:0xf bank_mask:0xf\n"
        "v_min_u32 v13, v13, v11\n"
        "v_bitop3_b32 v14, v14, v12, v150 bitop3:0xf6\n"
        "v_cmp_eq_u32 s[64:65], 0, v13\n"
        "v_cmp_ne_u32 vcc, 0, v14\n"
        "s_and_b32 s64, s64, 1\n"
        "s_add_u32 s33, s36, 1\n"
        "s_and_b32 s57, s33, 0xff\n"
        "ds_write_b128 v149, v[16:19] offset:0\n"
        "ds_write_b128 v149, v[20:23] offset:1024\n"
        "ds_write_b128 v149, v[24:27] offset:2048\n"
        "ds_write_b128 v149, v[28:31] offset:3072\n"
        "s_or_b32 s64, s64, vcc_lo\n"
        "s_or_b32 s64, s64, vcc_hi\n"
        "s_cbranch_scc1 P2_RETRY_%=\n"
        "P2_OK_%=:\n"
        "ds_write_b128 v149, v[32:35] offset:4096\n"
        "ds_write_b128 v149, v[36:39] offset:5120\n"
        "ds_write_b128 v149, v[40:43] offset:6144\n"
        "ds_write_b128 v149, v[44:47] offset:7168\n"
        "RED_%=:\n"
        "v_mov_b32 v6, s57\n"
        "s_movk_i32 s58, 0xff00\n"
        "s_waitcnt lgkmcnt(0)\n"
        "s_barrier\n"
        "ds_read_b128 v[16:19], v140 offset:0\n"
        "ds_read_b128 v[20:23], v140 offset:8192\n"
        "ds_read_b128 v[24:27], v140 offset:16384\n"
        "ds_read_b128 v[28:31], v140 offset:24576\n"
        "s_waitcnt lgkmcnt(2)\n"
        "v_pk_add_f32 v[16:17], v[16:17], v[20:21]\n"
        "v_pk_add_f32 v[18:19], v[18:19], v[22:23]\n"
        "s_waitcnt lgkmcnt(0)\n"
        "v_pk_add_f32 v[24:25], v[24:25], v[28:29]\n"
        "v_pk_add_f32 v[26:27], v[26:27], v[30:31]\n"
        "v_pk_add_f32 v[16:17], v[16:17], v[24:25]\n"
        "v_pk_add_f32 v[18:19], v[18:19], v[26:27]\n"
        "v_and_or_b32 v16, v16, s58, v6\n"
        "v_and_or_b32 v17, v17, s58, v6\n"
        "v_and_or_b32 v18, v18, s58, v6\n"
        "v_and_or_b32 v19, v19, s58, v6\n"
        "buffer_store_dwordx4 v[16:19], v141, s[24:27], s66 offen sc1\n"
        "s_cmp_lt_u32 s30, 4\n"
        "s_cbranch_scc0 LSTORE_%=\n"
        "ds_read_b32 v145, v146 offset:57344\n"
        "ds_read_b32 v20, v147 offset:0\n"
        "ds_read_b32 v21, v147 offset:8192\n"
        "ds_read_b32 v22, v147 offset:16384\n"
        "ds_read_b32 v23, v147 offset:24576\n"
        "s_waitcnt lgkmcnt(4)\n"
        "v_subrev_u32 v145, s60, v145\n"
        "v_cmp_gt_u32 s[54:55], s56, v145\n"
        "C_TW_%=:\n"
        "s_memrealtime s[64:65]\n"
        "s_waitcnt lgkmcnt(0)\n"
        "s_sub_u32 s64, s64, s68\n"
        "v_cmp_lt_u32 vcc, s64, v145\n"
        "s_and_b64 vcc, vcc, s[54:55]\n"
        "s_cbranch_scc1 C_TW_%=\n"
        "C_TRY_%=:\n"
        "buffer_load_dword v24, v146, s[24:27], s52 offen sc1\n"
        "buffer_load_dword v25, v146, s[24:27], s52 offen offset:1024 sc1\n"
        "buffer_load_dword v26, v146, s[24:27], s52 offen offset:2048 sc1\n"
        "buffer_load_dword v27, v146, s[24:27], s52 offen offset:3072 sc1\n"
        "buffer_load_dword v28, v146, s[24:27], s53 offen sc1\n"
        "buffer_load_dword v29, v146, s[24:27], s53 offen offset:1024 sc1\n"
        "buffer_load_dword v30, v146, s[24:27], s53 offen offset:2048 sc1\n"
        "v_add_f32 v32, v20, v21\n"
        "v_add_f32 v33, v22, v23\n"
        "v_add_f32 v32, v32, v33\n"
        "s_waitcnt vmcnt(6)\n"
        "v_xor_b32 v24, s57, v24\n"
        "v_add_f32 v32, v32, v24\n"
        "s_waitcnt vmcnt(5)\n"
        "v_xor_b32 v25, s57, v25\n"
        "v_add_f32 v32, v32, v25\n"
        "s_waitcnt vmcnt(4)\n"
        "v_xor_b32 v26, s57, v26\n"
        "v_add_f32 v32, v32, v26\n"
        "v_or3_b32 v10, v24, v25, v26\n"
        "s_waitcnt vmcnt(3)\n"
        "v_xor_b32 v27, s57, v27\n"
        "v_add_f32 v32, v32, v27\n"
        "s_waitcnt vmcnt(2)\n"
        "v_xor_b32 v28, s57, v28\n"
        "v_add_f32 v32, v32, v28\n"
        "v_or3_b32 v10, v10, v27, v28\n"
        "s_waitcnt vmcnt(1)\n"
        "v_xor_b32 v29, s57, v29\n"
        "v_add_f32 v32, v32, v29\n"
        "s_waitcnt vmcnt(0)\n"
        "v_xor_b32 v30, s57, v30\n"
        "v_add_f32 v32, v32, v30\n"
        "v_or3_b32 v10, v10, v29, v30\n"
        "v_cvt_pk_bf16_f32 v32, v32, v32\n"
        "v_and_b32 v10, 0xff, v10\n"
        "v_cmp_ne_u32 vcc, 0, v10\n"
        "s_cbranch_vccnz C_RETRY_%=\n"
        "C_OK_%=:\n"
        "buffer_store_short v32, v148, s[20:23], s49 offen\n"
        "s_or_b32 s56, s31, s30\n"
        "s_cbranch_scc1 DONE_%=\n"
        "v_mov_b32 v5, s33\n"
        "s_mov_b64 exec, 1\n"
        "buffer_store_dword v5, off, s[24:27], s39\n"
        "DONE_%=:\n"
        "s_endpgm\n"
        "LSTORE_%=:\n"
        "s_mov_b64 exec, 1\n"
        "buffer_store_dwordx4 v[132:135], off, s[24:27], s67\n"
        "buffer_store_dwordx3 v[136:138], off, s[24:27], s67 offset:16\n"
        "s_branch DONE_%=\n"
        "C_RETRY_%=:\n"
        "s_sub_u32 s62, s62, 1\n"
        "s_cbranch_scc1 C_OK_%=\n"
        "s_sleep 1\n"
        "s_branch C_TRY_%=\n"
        "P2_RETRY_%=:\n"
        "s_sub_u32 s62, s62, 1\n"
        "s_cbranch_scc1 P2_OK_%=\n"
        "s_sleep 1\n"
        "buffer_load_dwordx4 v[104:107], v2, s[24:27], s52 offen sc1\n"
        "buffer_load_dword v132, v148, s[24:27], s52 offen sc1\n"
        "buffer_load_dwordx4 v[108:111], v2, s[24:27], s52 offen offset:1280 sc1\n"
        "buffer_load_dword v133, v148, s[24:27], s52 offen offset:1280 sc1\n"
        "buffer_load_dwordx4 v[112:115], v2, s[24:27], s52 offen offset:2560 sc1\n"
        "buffer_load_dword v134, v148, s[24:27], s52 offen offset:2560 sc1\n"
        "buffer_load_dwordx4 v[116:119], v2, s[24:27], s52 offen offset:3840 sc1\n"
        "buffer_load_dword v135, v148, s[24:27], s52 offen offset:3840 sc1\n"
        "buffer_load_dwordx4 v[120:123], v2, s[24:27], s53 offen sc1\n"
        "buffer_load_dword v136, v148, s[24:27], s53 offen sc1\n"
        "buffer_load_dwordx4 v[124:127], v2, s[24:27], s53 offen offset:1280 sc1\n"
        "buffer_load_dword v137, v148, s[24:27], s53 offen offset:1280 sc1\n"
        "buffer_load_dwordx4 v[128:131], v2, s[24:27], s53 offen offset:2560 sc1\n"
        "buffer_load_dword v138, v148, s[24:27], s53 offen offset:2560 sc1\n"
        "s_branch P2_TRY_%=\n"
        "P2_%=:\n"
        "s_mov_b64 exec, s[54:55]\n"
        "buffer_load_dword v145, v144, s[24:27], 0 offen sc1\n"
        "s_mov_b64 exec, -1\n"
        "s_movk_i32 s56, 2000\n"
        "s_waitcnt vmcnt(0)\n"
        "v_subrev_u32 v145, s68, v145\n"
        "v_cmp_le_u32 vcc, s56, v145\n"
        "s_and_b64 vcc, vcc, s[54:55]\n"
        "s_cbranch_scc1 P2_NOHINT_%=\n"
        "P2_TW_%=:\n"
        "s_memrealtime s[64:65]\n"
        "s_waitcnt lgkmcnt(0)\n"
        "s_sub_u32 s64, s64, s68\n"
        "v_cmp_lt_u32 vcc, s64, v145\n"
        "s_and_b64 vcc, vcc, s[54:55]\n"
        "s_cbranch_scc1 P2_TW_%=\n"
        "s_branch P2_GO_%=\n"
        "P2_NOHINT_%=:\n"
        "s_sleep 4\n"
        "s_memrealtime s[64:65]\n"
        "s_waitcnt lgkmcnt(0)\n"
        "s_sub_u32 s64, s64, s68\n"
        "s_cmpk_lt_u32 s64, 1200\n"
        "s_cbranch_scc1 P2_%=\n"
        "s_branch P2_GO_%=\n"
        "GEGLU_END_%=:\n"
        :: "s"(__builtin_amdgcn_kernarg_segment_ptr()), "s"(blockIdx.x), "v"(threadIdx.x)
        : "s3", "s4", "s5", "s6", "s7", "s8", "s9", "s10", "s11", "s12", "s13", "s14", "s15", "s16", "s17", "s18", "s19", "s20", "s21", "s22", "s23", "s24", "s25", "s26", "s27", "s28", "s29", "s30", "s31", "s33", "s34", "s35", "s36", "s37", "s38", "s39", "s40", "s41", "s42", "s43", "s44", "s45", "s46", "s47", "s48", "s49", "s50", "s51", "s52", "s53", "s54", "s55", "s56", "s57", "s58", "s59", "s60", "s61", "s62", "s63", "s64", "s65", "s66", "s67", "s68", "s69", "s70", "s71", "v1", "v2", "v3", "v4", "v5", "v6", "v7", "v8", "v9", "v10", "v11", "v12", "v13", "v14", "v15", "v16", "v17", "v18", "v19", "v20", "v21", "v22", "v23", "v24", "v25", "v26", "v27", "v28", "v29", "v30", "v31", "v32", "v33", "v34", "v35", "v36", "v37", "v38", "v39", "v40", "v41", "v42", "v43", "v44", "v45", "v46", "v47", "v48", "v49", "v50", "v51", "v52", "v53", "v54", "v55", "v56", "v57", "v58", "v59", "v60", "v61", "v62", "v63", "v64", "v65", "v66", "v67", "v68", "v69", "v70", "v71", "v72", "v73", "v74", "v75", "v76", "v77", "v78", "v79", "v80", "v81", "v82", "v83", "v84", "v85", "v86", "v87", "v88", "v89", "v90", "v91", "v92", "v93", "v94", "v95", "v96", "v97", "v98", "v99", "v100", "v101", "v102", "v103", "v104", "v105", "v106", "v107", "v108", "v109", "v110", "v111", "v112", "v113", "v114", "v115", "v116", "v117", "v118", "v119", "v120", "v121", "v122", "v123", "v124", "v125", "v126", "v127", "v128", "v129", "v130", "v131", "v132", "v133", "v134", "v135", "v136", "v137", "v138", "v139", "v140", "v141", "v142", "v143", "v144", "v145", "v146", "v147", "v148", "v149", "v150", "v151", "vcc", "scc", "memory");
}

extern "C" void kernel_launch(void* const* d_in, const int* in_sizes, int n_in,
                              void* d_out, int out_size, void* d_ws, size_t ws_size,
                              hipStream_t stream) {
    (void)hipFuncSetAttribute((const void*)geglu_ffn, hipFuncAttributeMaxDynamicSharedMemorySize, LDS_BYTES);
    geglu_ffn<<<256, 512, LDS_BYTES, stream>>>((const u16*)d_in[0], (const u16*)d_in[1], (const u16*)d_in[2], (const u16*)d_in[3], (u16*)d_out, (char*)d_ws);
}
```
